```python
import torch

def _softmax(x, axis=-1):
    return torch.softmax(x, dim=axis)
def _gelu(x):
    return torch.nn.functional.gelu(x, approximate="tanh")
def _silu(x):
    return torch.nn.functional.silu(x)
def _sigmoid(x):
    return torch.sigmoid(x)
def _layernorm(x, axis=-1, eps=1e-5):
    m = x.mean(dim=axis, keepdim=True)
    v = x.var(dim=axis, keepdim=True, unbiased=False)
    return (x - m) / torch.sqrt(v + eps)
def _scatter_add(tgt, idx, src, axis=0):
    return tgt.clone().index_add_(axis, idx, src)
def _segment_sum(src, seg, n):
    out = torch.zeros((n,) + src.shape[1:], dtype=src.dtype)
    return out.index_add_(0, seg, src)
def _pad(x, pw):
    flat = tuple(v for pair in reversed(pw) for v in pair)
    return torch.nn.functional.pad(x, flat)

kernel_name = 'standalone__cheap_equivariant_egnn__graph'

def setup_inputs():

    _w = (lambda a: (lambda t: t.to(torch.bfloat16) if t.is_floating_point() else t)(torch.as_tensor(a)))
    N, deg, D, De = 10000, 64, 64, max(4, 1)
    E = N * deg
    return {
        "h": _w((lambda *s: torch.randn(*s, dtype=torch.bfloat16) * 0.2)(N, D)),
        "pos": _w((lambda *s: torch.randn(*s, dtype=torch.bfloat16) * 0.8)(N, 3)),
        "e_attr": _w((lambda *s: torch.randn(*s, dtype=torch.bfloat16) * 0.8)(E, De)),
        "src": _w((lambda lo, hi, *s: torch.randint(lo, hi, s))(0, N, E).to(torch.int64)),
        "dst": _w((lambda lo, hi, *s: torch.randint(lo, hi, s))(0, N, E).to(torch.int64)),
        "w_e": _w(0.1 * (lambda *s: torch.randn(*s, dtype=torch.bfloat16) * 0.8)(2 * D + 1 + De, D)),
        "w_x": _w(0.1 * (lambda *s: torch.randn(*s, dtype=torch.bfloat16) * 0.2)(D, 1)),
        "w_h": _w(0.1 * (lambda *s: torch.randn(*s, dtype=torch.bfloat16) * 0.2)(2 * D, D)),
    }

def reference(h, pos, e_attr, src, dst, w_e, w_x, w_h):
    N, D = h.shape[0], h.shape[1]
    diff = pos[dst] - pos[src]
    dist2 = torch.sum(diff * diff, axis=-1, keepdims=True)
    e_in = torch.concatenate([h[dst], h[src], dist2, e_attr], axis=-1)
    m_ij = _silu(e_in @ w_e)
    coord_scale = m_ij @ w_x
    pos_new = pos + _segment_sum(diff * coord_scale, dst, N)
    m_i = _segment_sum(m_ij, dst, N)
    h_new = h + _silu(torch.concatenate([h, m_i], axis=-1) @ w_h)
    return h_new, pos_new
```

```cpp
#include <hip/hip_runtime.h>
#include <hip/hip_bf16.h>
typedef unsigned short u16;
__device__ __forceinline__ float b2f(u16 x){ return __uint_as_float(((unsigned)x)<<16); }
__device__ __forceinline__ u16 f2b(float f){ unsigned u=__float_as_uint(f); u += 0x7fffu + ((u>>16)&1u); return (u16)(u>>16); }
__device__ __forceinline__ float rb(float f){ return b2f(f2b(f)); }

__global__ void pre_k(const u16* h, const u16* we, float* A, float* B, int N){
  int n = blockIdx.x; int f = threadIdx.x;
  float a=0,b=0;
  for(int k=0;k<64;k++){ float x=b2f(h[n*64+k]); a+=x*b2f(we[k*64+f]); b+=x*b2f(we[(64+k)*64+f]); }
  A[n*64+f]=a; B[n*64+f]=b;
}
__global__ void edge_k(const float* A,const float* B,const u16* pos,const u16* ea,const int* src,const int* dst,const u16* we,const u16* wx,float* M,float* P,int E){
  int e = blockIdx.x*4 + (threadIdx.x>>6); int f = threadIdx.x&63;
  if (e>=E) return;
  int s=src[e], d=dst[e];
  float dx=rb(b2f(pos[d*3])-b2f(pos[s*3])), dy=rb(b2f(pos[d*3+1])-b2f(pos[s*3+1])), dz=rb(b2f(pos[d*3+2])-b2f(pos[s*3+2]));
  float d2 = rb(rb(dx*dx)+rb(dy*dy)+rb(dz*dz));
  float x = A[d*64+f]+B[s*64+f]+d2*b2f(we[128*64+f]);
  for(int k=0;k<4;k++) x += b2f(ea[e*4+k])*b2f(we[(129+k)*64+f]);
  float m = x/(1.f+__expf(-x));
  m = rb(m);
  atomicAdd(&M[d*64+f], m);
  float c = m*b2f(wx[f]);
  for(int o=32;o;o>>=1) c += __shfl_xor(c,o);
  if (f<3){ float dd = f==0?dx:(f==1?dy:dz); atomicAdd(&P[d*4+f], dd*c); }
}
__global__ void node_k(const u16* h,const u16* pos,const u16* wh,const float* M,const float* P,u16* out,int N){
  int n=blockIdx.x, f=threadIdx.x;
  float z=0;
  for(int k=0;k<64;k++){ z+=b2f(h[n*64+k])*b2f(wh[k*64+f]); z+=rb(M[n*64+k])*b2f(wh[(64+k)*64+f]); }
  float s = z/(1.f+__expf(-z));
  out[n*64+f]=f2b(b2f(h[n*64+f])+s);
  if(f<3) out[N*64+n*3+f]=f2b(b2f(pos[n*3+f])+P[n*4+f]);
}
extern "C" void kernel_launch(void* const* d_in, const int* in_sizes, int n_in, void* d_out, int out_size, void* d_ws, size_t ws_size, hipStream_t stream){
  const u16* h=(const u16*)d_in[0]; const u16* pos=(const u16*)d_in[1]; const u16* ea=(const u16*)d_in[2];
  const int* src=(const int*)d_in[3]; const int* dst=(const int*)d_in[4];
  const u16* we=(const u16*)d_in[5]; const u16* wx=(const u16*)d_in[6]; const u16* wh=(const u16*)d_in[7];
  int N=in_sizes[0]/64, E=in_sizes[3];
  float* M=(float*)d_ws; float* P=M+N*64; float* A=P+N*4; float* B=A+N*64;
  hipMemsetAsync(M,0,(size_t)N*68*4,stream);
  pre_k<<<N,64,0,stream>>>(h,we,A,B,N);
  edge_k<<<(E+3)/4,256,0,stream>>>(A,B,pos,ea,src,dst,we,wx,M,P,E);
  node_k<<<N,64,0,stream>>>(h,pos,wh,M,P,(u16*)d_out,N);
}
```

```cpp
#include <hip/hip_runtime.h>
#include <hip/hip_bf16.h>
#include <stdint.h>

typedef unsigned short u16;
typedef unsigned int u32;
typedef unsigned long long u64;
typedef __attribute__((ext_vector_type(16))) float f32x16;
typedef __attribute__((ext_vector_type(4))) float f32x4;
typedef __attribute__((ext_vector_type(2))) float f32x2;
typedef __attribute__((ext_vector_type(8))) __bf16 bf16x8;
typedef __attribute__((ext_vector_type(2))) __bf16 bf16x2;
typedef __attribute__((ext_vector_type(4))) short s16x4;
typedef __attribute__((ext_vector_type(4))) u32 u32x4;
typedef __attribute__((ext_vector_type(2))) u32 u32x2;
#define LAS __attribute__((address_space(3)))
#define DI __device__ __forceinline__

constexpr int NN = 10000, EE = 640000, NBK = 256, CH = 2500, CHS = 2560;
constexpr float NLOG2E = -1.4426950408889634f, NLN2 = -0.6931471805599453f;

constexpr size_t WS_IDX = 0;
constexpr size_t WS_EA  = WS_IDX + (size_t)NBK * CHS * 4;
constexpr size_t WS_TAB = WS_EA + (size_t)NBK * CHS * 8;
constexpr size_t WS_BP  = WS_TAB + (size_t)NBK * NBK * 4;
constexpr size_t WS_AP  = WS_BP + (size_t)NN * 128;
constexpr size_t WS_P4  = WS_AP + (size_t)NN * 128;
constexpr size_t WS_WHF = WS_P4 + (size_t)NN * 8;
constexpr size_t WS_WP  = WS_WHF + 16384;
constexpr size_t WS_END = WS_WP + 1024;

DI float b2f(u32 x) { return __uint_as_float(x << 16); }
DI float bhi(u32 x) { return __uint_as_float(x & 0xffff0000u); }
DI u32 pk2(float a, float b) { f32x2 v = {a, b}; return __builtin_bit_cast(u32, __builtin_convertvector(v, bf16x2)); }
DI float rbf(float a) { return b2f(pk2(a, 0.f) & 0xffffu); }
DI int first_node(int b) { return (b * 625 + 15) >> 4; }

template <int CTRL, int RM> DI u32 dpp0(u32 x) { return (u32)__builtin_amdgcn_update_dpp(0, (int)x, CTRL, RM, 0xf, false); }
DI u32 scan_add(u32 x) {
  x += dpp0<0x111, 0xf>(x); x += dpp0<0x112, 0xf>(x); x += dpp0<0x114, 0xf>(x); x += dpp0<0x118, 0xf>(x);
  x += dpp0<0x142, 0xa>(x); x += dpp0<0x143, 0xc>(x); return x;
}
DI u32 umax(u32 a, u32 b) { return a > b ? a : b; }
DI u32 scan_max(u32 x) {
  x = umax(x, dpp0<0x111, 0xf>(x)); x = umax(x, dpp0<0x112, 0xf>(x)); x = umax(x, dpp0<0x114, 0xf>(x)); x = umax(x, dpp0<0x118, 0xf>(x));
  x = umax(x, dpp0<0x142, 0xa>(x)); x = umax(x, dpp0<0x143, 0xc>(x)); return x;
}
DI u32x2 tr_read(u32 addr) { return __builtin_bit_cast(u32x2, __builtin_amdgcn_ds_read_tr16_b64_v4i16((LAS s16x4*)addr)); }
template <typename T> DI T lds_ld(u32 addr) { return *(LAS T*)addr; }
template <typename T> DI void lds_st(u32 addr, T v) { *(LAS T*)addr = v; }
DI int med3(int a, int lo, int hi) { int r; asm("v_med3_i32 %0, %1, %2, %3" : "=v"(r) : "v"(a), "v"(lo), "v"(hi)); return r; }

constexpr int K1T = 640, K1NODEBLK = 63;
__global__ __launch_bounds__(K1T) void egnn_k1(const u16* __restrict__ h, const u16* __restrict__ pos, const u32x4* __restrict__ ea4,
                                               const u32x4* __restrict__ src4, const u32x4* __restrict__ dst4,
                                               const u16* __restrict__ we, const u16* __restrict__ wh, char* __restrict__ ws) {
  __shared__ __attribute__((aligned(16))) char smem[CHS * 12 + 2048];
  const int t = threadIdx.x, lane = t & 63;
  if (blockIdx.x < NBK) {
    u32* hist = (u32*)(smem + CHS * 12);
    u32* offs = hist + 256;
    u32* sidx = (u32*)smem;
    u64* sea = (u64*)(smem + CHS * 4);
    const int c = blockIdx.x;
    const bool act = t < CH / 4;
    const int q = c * (CH / 4) + (act ? t : 0);
    u32x4 d = dst4[q], s = src4[q], e0 = ea4[2 * q], e1 = ea4[2 * q + 1];
    if (t < 256) hist[t] = 0;
    __syncthreads();
    u32 bk[4], rk[4];
#pragma unroll
    for (int k = 0; k < 4; ++k) { bk[k] = (d[k] * 16u) / 625u; }
    if (act) {
#pragma unroll
      for (int k = 0; k < 4; ++k) rk[k] = atomicAdd(&hist[bk[k]], 1u);
    }
    __syncthreads();
    {
      u32x4 cn = *(u32x4*)(hist + 4 * lane);
      u32 tot = cn[0] + cn[1] + cn[2] + cn[3];
      u32 ex = scan_add(tot) - tot;
      u32x4 o = {ex, ex + cn[0], ex + cn[0] + cn[1], ex + cn[0] + cn[1] + cn[2]};
      *(u32x4*)(offs + 4 * lane) = o;
      if (t < 64) {
        u32x4 te = {o[0] | (cn[0] << 16), o[1] | (cn[1] << 16), o[2] | (cn[2] << 16), o[3] | (cn[3] << 16)};
        *(u32x4*)(ws + WS_TAB + (size_t)c * 1024 + lane * 16) = te;
      }
    }
    asm volatile("" ::: "memory");
    if (act) {
#pragma unroll
      for (int k = 0; k < 4; ++k) rk[k] += offs[bk[k]];
      u64 ev[4] = {e0[0] | ((u64)e0[1] << 32), e0[2] | ((u64)e0[3] << 32), e1[0] | ((u64)e1[1] << 32), e1[2] | ((u64)e1[3] << 32)};
#pragma unroll
      for (int k = 0; k < 4; ++k) {
        const u32 p = rk[k];
        sidx[p] = (s[k] << 7) | (d[k] - (u32)first_node(bk[k]));
        sea[p] = ev[k];
      }
    }
    __syncthreads();
    if (act) {
      *(u32x4*)(ws + WS_IDX + (size_t)c * CHS * 4 + t * 16) = *(u32x4*)(smem + t * 16);
      *(u32x4*)(ws + WS_EA + (size_t)c * CHS * 8 + t * 16) = *(u32x4*)(smem + CHS * 4 + t * 16);
      *(u32x4*)(ws + WS_EA + (size_t)c * CHS * 8 + 10000 + t * 16) = *(u32x4*)(smem + CHS * 4 + 10000 + t * 16);
    }
  } else if (blockIdx.x < NBK + K1NODEBLK) {
    const int w = t >> 6;
    const int tile = (blockIdx.x - NBK) * 10 + w;
    const bool tv = tile < NN / 16;
    const int n = (tv ? tile : 0) * 16 + (lane & 15), g = lane >> 4;
    u32x4 hf0 = *(const u32x4*)(h + n * 64 + 8 * g), hf1 = *(const u32x4*)(h + n * 64 + 32 + 8 * g);
    for (int i = t; i < 1024; i += K1T) *(u32x4*)(smem + i * 16) = *(const u32x4*)(we + i * 8);
    if (g == 0 && tv) {
      u32 x = pos[n * 3], y = pos[n * 3 + 1], z = pos[n * 3 + 2];
      u32x2 pv = {x | (y << 16), z};
      *(u32x2*)(ws + WS_P4 + n * 8) = pv;
    }
    __syncthreads();
    const u32 sb = (u32)(size_t)smem;
    const int q = (lane >> 2) & 3, p = lane & 3;
#pragma unroll
    for (int m = 0; m < 2; ++m) {
      u32 o[8];
#pragma unroll
      for (int T = 0; T < 4; ++T) {
        f32x4 acc = {0.f, 0.f, 0.f, 0.f};
#pragma unroll
        for (int ks = 0; ks < 2; ++ks) {
          const u32 a0 = sb + (u32)((64 * m + 32 * ks + 8 * g + q) * 128 + (16 * p + 4 * T) * 2);
          u32x2 lo = tr_read(a0), hi = tr_read(a0 + 512);
          u32x4 af = {lo[0], lo[1], hi[0], hi[1]};
          acc = __builtin_amdgcn_mfma_f32_16x16x32_bf16(__builtin_bit_cast(bf16x8, af), __builtin_bit_cast(bf16x8, ks ? hf1 : hf0), acc, 0, 0, 0);
        }
        o[2 * T] = pk2(acc[0] * NLOG2E, acc[1] * NLOG2E);
        o[2 * T + 1] = pk2(acc[2] * NLOG2E, acc[3] * NLOG2E);
      }
      if (tv) {
        char* dstp = ws + (m ? WS_BP : WS_AP) + (size_t)n * 128 + g * 32;
        u32x4 v0 = {o[0], o[1], o[2], o[3]}, v1 = {o[4], o[5], o[6], o[7]};
        *(u32x4*)dstp = v0; *(u32x4*)(dstp + 16) = v1;
      }
    }
  } else {
    u16* whf = (u16*)(ws + WS_WHF);
    for (int i = t; i < 8192; i += K1T) {
      const int j = i & 7, l = (i >> 3) & 63, ks = (i >> 9) & 3, T = i >> 11;
      whf[i] = wh[(32 * ks + 8 * (l >> 4) + j) * 64 + 16 * T + (l & 15)];
    }
    if (t < 512) {
      const int r = t >> 6, f = t & 63;
      float v = 0.f;
      if (r < 4) v = b2f(we[(129 + r) * 64 + f]) * NLOG2E;
      else if (r < 6) { float x = b2f(we[128 * 64 + f]) * NLOG2E; float hi = rbf(x); v = r == 4 ? hi : x - hi; }
      ((u16*)(ws + WS_WP))[t] = (u16)(pk2(v, 0.f) & 0xffffu);
    }
  }
}

constexpr int NW = 12, SW = (NBK + NW - 1) / NW, PASSES = 5, CAP = 2816, QSAFE = 192;
constexpr int STGS = 1088, STGW = 4 * STGS, MROW = 68;
constexpr u32 L_STG = 0;
constexpr u32 L_ZERO = L_STG + NW * STGW;
constexpr u32 L_REC = L_ZERO + 3328;
constexpr u32 L_DV = L_REC + CAP * 16;
constexpr u32 L_S = L_DV + (CAP + 48) * 8;
constexpr u32 L_M = L_S + (CAP + 64) * 4;
constexpr u32 L_P = L_M + 40 * MROW * 4;
constexpr u32 L_G = L_P + 40 * 48 * 4;
constexpr u32 L_OH = L_G + 48 * 128;
constexpr u32 L_PD = L_OH + 128;
constexpr u32 L_CNT = L_PD + 512;
constexpr u32 L_FLG = L_CNT + 256;
constexpr u32 L_END = L_FLG + 16;
static_assert(L_OH % 128 == 0 && L_ZERO % 256 == 0 && L_END <= 163840, "lds layout");
static_assert(QSAFE * NW <= CAP && QSAFE <= PASSES * 64, "safe round size");

__global__ __launch_bounds__(NW * 64) void egnn_k2(const u16* __restrict__ h, const u16* __restrict__ pos, const u16* __restrict__ wx,
                                                   const char* __restrict__ ws, u16* __restrict__ out) {
  extern __shared__ __attribute__((aligned(16))) char lds_[];
  const int tid = threadIdx.x, lane = tid & 63;
  const int w = __builtin_amdgcn_readfirstlane(tid >> 6);
  const int b = (blockIdx.x & 7) * 32 + (blockIdx.x >> 3);
  const int n0 = first_node(b), nn = first_node(b + 1) - n0;
  const u32* idxS = (const u32*)(ws + WS_IDX);
  const u32x2* eaS = (const u32x2*)(ws + WS_EA);
  const u32x2* pos4 = (const u32x2*)(ws + WS_P4);
  const char* Bp = ws + WS_BP;
  const u32 stg = L_STG + (u32)w * STGW;

  const int seg = w * SW + lane;
  const bool sv = lane < SW && seg < NBK;
  u32 te = ((const u32*)(ws + WS_TAB))[(sv ? seg : 0) * NBK + b];
  {
    u32x4 gv = {0, 0, 0, 0};
    if (tid < 320) { int r = tid >> 3; r = r < nn ? r : 0; gv = *(const u32x4*)(ws + WS_AP + (size_t)(n0 + r) * 128 + (tid & 7) * 16); }
    else if (tid < 384) gv = *(const u32x4*)(ws + WS_WP + (tid - 320) * 16);
    u32x2 pdv = {0, 0};
    if (tid >= 384 && tid < 384 + 64) { int r = tid - 384; pdv = pos4[n0 + (r < nn ? r : 0)]; }
    const u32x4 z4 = {0, 0, 0, 0};
    for (u32 i = tid; i < 3328 / 16; i += NW * 64) lds_st<u32x4>(L_ZERO + i * 16, z4);
    for (u32 i = tid; i < (L_G - L_M) / 16; i += NW * 64) lds_st<u32x4>(L_M + i * 16, z4);
    if (tid < 64) lds_st<u32>(L_CNT + tid * 4, 0u);
    if (tid == 0) lds_st<u32>(L_FLG, 0u);
    if (tid < 32) lds_st<u32>(L_OH + tid * 4, ((tid >> 3) == (tid & 3)) ? (((tid >> 2) & 1) ? 0x3F800000u : 0x3F80u) : 0u);
    if (tid < 384) lds_st<u32x4>(L_G + tid * 16, gv);
    else if (tid < 384 + 64) lds_st<u32x2>(L_PD + (tid - 384) * 8, pdv);
  }
  const float wx0 = b2f(wx[lane & 31]) * NLN2, wx1 = b2f(wx[32 + (lane & 31)]) * NLN2;

  const u32 scnt = sv ? (te >> 16) : 0u;
  const u32 sincl = scan_add(scnt);
  const u32 sexcl = sincl - scnt;
  const u32 Tw = __builtin_amdgcn_readlane(sincl, 63);
  const u32 delta = (u32)seg * CHS + (te & 0xffffu) - sexcl + 4096u;

  u32 Q = PASSES * 64, vbase = 0;
  bool firstpass = true, synced = false;
  for (;;) {
    lds_st<u32x4>(stg + lane * 16, u32x4{0, 0, 0, 0});
    if (lane < 16) lds_st<u32x4>(stg + 1024 + lane * 16, u32x4{0, 0, 0, 0});
    if (scnt > 0 && sexcl < vbase + Q) {
      u32 pp = sexcl > vbase ? sexcl - vbase : 0u;
      __hip_atomic_fetch_max((LAS u32*)(stg + pp * 4), delta, __ATOMIC_RELAXED, __HIP_MEMORY_SCOPE_WAVEFRONT);
    }
    asm volatile("" ::: "memory");
    const u32 vend = Tw < vbase + Q ? Tw : vbase + Q;
    u32 ridx[PASSES], rrank[PASSES];
    u32x2 rea[PASSES], rps[PASSES];
    {
      u32 carry = 0;
#pragma unroll
      for (int k = 0; k < PASSES; ++k) {
        u32 dl = lds_ld<u32>(stg + (64 * k + lane) * 4);
        dl = umax(scan_max(dl), carry);
        carry = __builtin_amdgcn_readlane(dl, 63);
        const u32 v = vbase + 64 * k + lane;
        const u32 ri = v < vend ? v + dl - 4096u : 0u;
        ridx[k] = idxS[ri];
        rea[k] = eaS[ri];
      }
    }
    if (!synced) { __syncthreads(); synced = true; }
    if (firstpass && Tw > Q && lane == 0) lds_st<u32>(L_FLG, 1u);
#pragma unroll
    for (int k = 0; k < PASSES; ++k) rps[k] = pos4[ridx[k] >> 7];
#pragma unroll
    for (int k = 0; k < PASSES; ++k) {
      if (vbase + 64 * k + lane < vend)
        rrank[k] = __hip_atomic_fetch_add((LAS u32*)(L_CNT + (ridx[k] & 63u) * 4), 1u, __ATOMIC_RELAXED, __HIP_MEMORY_SCOPE_WORKGROUP);
    }
    u32 rd2[PASSES];
#pragma unroll
    for (int k = 0; k < PASSES; ++k) {
      const u32x2 pd = lds_ld<u32x2>(L_PD + (ridx[k] & 63u) * 8);
      const u32 dxy = pk2(b2f(pd[0]) - b2f(rps[k][0]), bhi(pd[0]) - bhi(rps[k][0]));
      const u32 dz = pk2(b2f(pd[1]) - b2f(rps[k][1]), 0.f);
      const float x = b2f(dxy), y = bhi(dxy), z = b2f(dz);
      const u32 sxy = pk2(x * x, y * y), sz = pk2(z * z, 0.f);
      const float d2 = b2f(sxy) + bhi(sxy) + b2f(sz);
      rd2[k] = pk2(d2, d2);
      rps[k][0] = 0x3F80u | (dxy << 16);
      rps[k][1] = (dxy >> 16) | (dz << 16);
    }
    __syncthreads();
    const u32 cn = lds_ld<u32>(L_CNT + lane * 4);
    const u32 nincl = scan_add(cn);
    const u32 nstart = nincl - cn;
    const u32 total = __builtin_amdgcn_readlane(nincl, 63);
    if (firstpass) {
      const u32 flg = __builtin_amdgcn_readfirstlane(lds_ld<u32>(L_FLG));
      firstpass = false;
      if (flg || total > (u32)CAP) {
        __syncthreads();
        if (tid < 64) lds_st<u32>(L_CNT + tid * 4, 0u);
        if (tid == 0) lds_st<u32>(L_FLG, 0u);
        __syncthreads();
        if (lane == 0) __hip_atomic_fetch_max((LAS u32*)L_FLG, Tw, __ATOMIC_RELAXED, __HIP_MEMORY_SCOPE_WORKGROUP);
        Q = QSAFE;
        continue;
      }
    }
#pragma unroll
    for (int k = 0; k < PASSES; ++k) {
      const u32 nl = ridx[k] & 63u;
      const u32 st = (u32)__builtin_amdgcn_ds_bpermute((int)(nl * 4), (int)nstart);
      if (vbase + 64 * k + lane < vend) {
        const u32 slot = st + rrank[k];
        lds_st<u32x4>(L_REC + slot * 16, u32x4{rea[k][0], rea[k][1], rd2[k], 0u});
        lds_st<u32x2>(L_DV + (slot + nl + 1) * 8, rps[k]);
        lds_st<u32>(L_S + slot * 4, (ridx[k] & ~127u) | ((nl & 7u) << 4));
      }
    }
    if (w == 0) { if (lane <= 41) lds_st<u32x2>(L_DV + (nstart + lane) * 8, u32x2{0, 0}); }
    if (w == 1) { lds_st<u32>(L_S + (total + lane) * 4, 0u); if (lane < 32 && total + lane < (u32)CAP) lds_st<u32x4>(L_REC + (total + lane) * 16, u32x4{0, 0, 0, 0}); }
    __syncthreads();

    const int ntiles = (int)(total + 31) >> 5;
    const int tb = (ntiles * w) / NW, tend = (ntiles * (w + 1)) / NW;
    const int hh = lane >> 5, blk = (lane >> 4) & 1, q = (lane >> 2) & 3, p = lane & 3;
    const u32 cbase = (q & 1) ? stg + 128 * hh + 32 * blk + 8 * p + (q >> 1) * STGS : L_ZERO + 128 * (1 - hh);
    const u32 maskh = hh ? 0x70u : 0u, fstep = hh ? 0u : 512u;
    const u32 lanepart = (lane & 7) * 16;
    u32 sbeg = (u32)tb * 32;
    const u32 send = (u32)tend * 32 < total ? (u32)tend * 32 : total;
    while (sbeg < send) {
      const int base = __builtin_popcountll(__ballot(nstart <= sbeg && lane < nn)) - 1;
      const int bend = base + 8 < nn ? base + 8 : nn;
      const u32 wend_ = __builtin_amdgcn_readlane(nstart, bend);
      const u32 wend = wend_ < send ? wend_ : send;
      const int t0 = (int)(sbeg >> 5), t1 = (int)((wend + 31) >> 5);
      bf16x8 wf[2];
#pragma unroll
      for (int t = 0; t < 2; ++t) {
        u32x2 r[2];
#pragma unroll
        for (int u = 0; u < 2; ++u) {
          int nd = base + ((4 * u + q - base) & 7); nd = nd < 40 ? nd : 0;
          const int row = hh ? nd : 40 + 4 * u + q;
          r[u] = tr_read(L_G + row * 128 + 64 * t + 32 * blk + 8 * p);
        }
        wf[t] = __builtin_bit_cast(bf16x8, u32x4{r[0][0], r[0][1], r[1][0], r[1][1]});
      }
      int np = base + ((4 * blk + p - base) & 7); np = np < nn ? np : nn;
      const int lo = (int)L_DV + 8 * ((int)__builtin_amdgcn_ds_bpermute(np * 4, (int)nstart) + np);
      const int hi = (int)L_DV + 8 * ((int)__builtin_amdgcn_ds_bpermute(np * 4 + 4, (int)nstart) + np + 1);
      int da = (int)L_DV + 8 * (t0 * 32 + 4 * hh + q + np + 1);
      u32 fb = hh ? L_OH : L_REC + (u32)(t0 * 32 + (lane & 31)) * 16;
      u32 s4a = L_S + (u32)(t0 * 32 + 4 * (lane >> 3)) * 4, s1a = L_S + (u32)(t0 * 32 + (lane & 31)) * 4;
      f32x16 acc0, acc1;
#pragma unroll
      for (int i = 0; i < 16; ++i) { acc0[i] = 0.f; acc1[i] = 0.f; }
      u32 s1 = lds_ld<u32>(s1a);
      {
        const u32x4 s4 = lds_ld<u32x4>(s4a);
#pragma unroll
        for (int i = 0; i < 4; ++i)
          __builtin_amdgcn_global_load_lds((const __attribute__((address_space(1))) void*)(Bp + ((s4[i] & ~127u) | lanepart)), (LAS void*)(stg + i * STGS), 16, 0, 0);
      }
      for (int t = t0; t < t1; ++t) {
        asm volatile("s_waitcnt vmcnt(0)" ::: "memory");
        f32x16 x0, x1;
#pragma unroll
        for (int k = 0; k < 8; ++k) {
          const u32 off = 2 * STGS * (k & 1) + 256 * (k >> 1);
          const u32x2 a = tr_read(cbase + off), c = tr_read(cbase + off + 64);
          x0[2 * k] = __uint_as_float(a[0]); x0[2 * k + 1] = __uint_as_float(a[1]);
          x1[2 * k] = __uint_as_float(c[0]); x1[2 * k + 1] = __uint_as_float(c[1]);
        }
        const u32x4 ff = lds_ld<u32x4>((s1 & maskh) | fb);
        u32x2 a2[4];
#pragma unroll
        for (int i = 0; i < 4; ++i) a2[i] = tr_read((u32)med3(da, lo - 64 * i, hi - 64 * i) + 64 * i);
        s4a += 128; s1a += 128; fb += fstep; da += 256;
        s1 = lds_ld<u32>(s1a);
        const u32x4 s4 = lds_ld<u32x4>(s4a);
        asm volatile("s_waitcnt lgkmcnt(0)" ::: "memory");
        if (t + 1 < t1) {
#pragma unroll
          for (int i = 0; i < 4; ++i)
            __builtin_amdgcn_global_load_lds((const __attribute__((address_space(1))) void*)(Bp + ((s4[i] & ~127u) | lanepart)), (LAS void*)(stg + i * STGS), 16, 0, 0);
        }
        x0 = __builtin_amdgcn_mfma_f32_32x32x16_bf16(__builtin_bit_cast(bf16x8, ff), wf[0], x0, 0, 0, 0);
        x1 = __builtin_amdgcn_mfma_f32_32x32x16_bf16(__builtin_bit_cast(bf16x8, ff), wf[1], x1, 0, 0, 0);
        const bf16x8 sa0 = __builtin_bit_cast(bf16x8, u32x4{a2[0][0], a2[0][1], a2[1][0], a2[1][1]});
        const bf16x8 sa1 = __builtin_bit_cast(bf16x8, u32x4{a2[2][0], a2[2][1], a2[3][0], a2[3][1]});
#pragma unroll
        for (int hf = 0; hf < 2; ++hf) {
          const f32x16& x = hf ? x1 : x0;
          u32 m[8];
#pragma unroll
          for (int i = 0; i < 8; ++i) {
            const f32x2 xv = {x[2 * i], x[2 * i + 1]};
            f32x2 e = {__builtin_amdgcn_exp2f(xv[0]), __builtin_amdgcn_exp2f(xv[1])};
            e += 1.0f;
            const f32x2 r = {__builtin_amdgcn_rcpf(e[0]), __builtin_amdgcn_rcpf(e[1])};
            const f32x2 mv = xv * r;
            m[i] = pk2(mv[0], mv[1]);
          }
          f32x16& acc = hf ? acc1 : acc0;
          acc = __builtin_amdgcn_mfma_f32_32x32x16_bf16(sa0, __builtin_bit_cast(bf16x8, u32x4{m[0], m[1], m[2], m[3]}), acc, 0, 0, 0);
          acc = __builtin_amdgcn_mfma_f32_32x32x16_bf16(sa1, __builtin_bit_cast(bf16x8, u32x4{m[4], m[5], m[6], m[7]}), acc, 0, 0, 0);
        }
      }
      const int lastn = __builtin_popcountll(__ballot(nstart < wend && lane < nn)) - 1;
#pragma unroll
      for (int g4 = 0; g4 < 4; ++g4) {
        const int nd = base + ((2 * g4 + hh - base) & 7);
        const bool on = nd <= lastn;
        if (__ballot(on)) {
          float pv[3];
#pragma unroll
          for (int c = 0; c < 3; ++c) {
            float v = acc0[4 * g4 + 1 + c] * wx0 + acc1[4 * g4 + 1 + c] * wx1;
            v += __uint_as_float((u32)__builtin_amdgcn_update_dpp(0, (int)__float_as_uint(v), 0xB1, 0xf, 0xf, false));
            pv[c] = v;
          }
          if (on) {
            const u32 ma = L_M + (u32)(nd * MROW + (lane & 31)) * 4;
            __hip_atomic_fetch_add((LAS float*)ma, acc0[4 * g4], __ATOMIC_RELAXED, __HIP_MEMORY_SCOPE_WORKGROUP);
            __hip_atomic_fetch_add((LAS float*)(ma + 128), acc1[4 * g4], __ATOMIC_RELAXED, __HIP_MEMORY_SCOPE_WORKGROUP);
            if (!(lane & 1)) {
              const u32 pa = L_P + (u32)(nd * 48 + ((lane & 31) >> 1)) * 4;
#pragma unroll
              for (int c = 0; c < 3; ++c) __hip_atomic_fetch_add((LAS float*)(pa + 64 * c), pv[c], __ATOMIC_RELAXED, __HIP_MEMORY_SCOPE_WORKGROUP);
            }
          }
        }
      }
      sbeg = wend;
    }
    if (Q == PASSES * 64) break;
    vbase += Q;
    const u32 tmax = __builtin_amdgcn_readfirstlane(lds_ld<u32>(L_FLG));
    if (vbase >= tmax) break;
    __syncthreads();
    if (tid < 64) lds_st<u32>(L_CNT + tid * 4, 0u);
    __syncthreads();
  }

  {
    const int nt = w >> 2, T = w & 3, g = lane >> 4;
    const int nl = nt * 16 + (lane & 15);
    const bool nv = nl < nn;
    const int n = n0 + (nv ? nl : 0);
    u32x4 wfr[4];
#pragma unroll
    for (int ks = 0; ks < 4; ++ks) wfr[ks] = *(const u32x4*)(ws + WS_WHF + ((T * 4 + ks) * 64 + lane) * 16);
    const u32x4 hf0 = *(const u32x4*)(h + n * 64 + 8 * g), hf1 = *(const u32x4*)(h + n * 64 + 32 + 8 * g);
    const u32x2 hr = *(const u32x2*)(h + n * 64 + 16 * T + 4 * g);
    const int pi = tid >> 2;
    const bool pvld = pi < nn * 3;
    const u32 pold = pos[n0 * 3 + (pvld ? pi : 0)];
    f32x4 z = {0.f, 0.f, 0.f, 0.f};
    z = __builtin_amdgcn_mfma_f32_16x16x32_bf16(__builtin_bit_cast(bf16x8, wfr[0]), __builtin_bit_cast(bf16x8, hf0), z, 0, 0, 0);
    z = __builtin_amdgcn_mfma_f32_16x16x32_bf16(__builtin_bit_cast(bf16x8, wfr[1]), __builtin_bit_cast(bf16x8, hf1), z, 0, 0, 0);
    __syncthreads();
#pragma unroll
    for (int ks = 0; ks < 2; ++ks) {
      const u32 ma = L_M + (u32)((nv ? nl : 0) * MROW + 32 * ks + 8 * g) * 4;
      const f32x4 a = lds_ld<f32x4>(ma), c = lds_ld<f32x4>(ma + 16);
      const u32x4 mf = {pk2(a[0] * NLN2, a[1] * NLN2), pk2(a[2] * NLN2, a[3] * NLN2), pk2(c[0] * NLN2, c[1] * NLN2), pk2(c[2] * NLN2, c[3] * NLN2)};
      z = __builtin_amdgcn_mfma_f32_16x16x32_bf16(__builtin_bit_cast(bf16x8, wfr[2 + ks]), __builtin_bit_cast(bf16x8, mf), z, 0, 0, 0);
    }
    float o[4];
#pragma unroll
    for (int i = 0; i < 4; ++i) o[i] = z[i] * __builtin_amdgcn_rcpf(1.f + __builtin_amdgcn_exp2f(z[i] * NLOG2E));
    if (nv) {
      const u32x2 ov = {pk2(b2f(hr[0]) + o[0], bhi(hr[0]) + o[1]), pk2(b2f(hr[1]) + o[2], bhi(hr[1]) + o[3])};
      *(u32x2*)(out + n * 64 + 16 * T + 4 * g) = ov;
    }
    const f32x4 pp = lds_ld<f32x4>(L_P + (u32)((pvld ? pi : 0) * 16 + (tid & 3) * 4) * 4);
    float ps = (pp[0] + pp[1]) + (pp[2] + pp[3]);
    ps += __uint_as_float((u32)__builtin_amdgcn_update_dpp(0, (int)__float_as_uint(ps), 0xB1, 0xf, 0xf, false));
    ps += __uint_as_float((u32)__builtin_amdgcn_update_dpp(0, (int)__float_as_uint(ps), 0x4E, 0xf, 0xf, false));
    if (pvld && (tid & 3) == 0) out[NN * 64 + n0 * 3 + pi] = (u16)(pk2(b2f(pold) + ps, 0.f) & 0xffffu);
  }
}

extern "C" void kernel_launch(void* const* d_in, const int* in_sizes, int n_in, void* d_out, int out_size, void* d_ws, size_t ws_size,
                              hipStream_t stream) {
  const u16* h = (const u16*)d_in[0]; const u16* pos = (const u16*)d_in[1];
  const u16* we = (const u16*)d_in[5]; const u16* wx = (const u16*)d_in[6]; const u16* wh = (const u16*)d_in[7];
  (void)hipFuncSetAttribute((const void*)egnn_k2, hipFuncAttributeMaxDynamicSharedMemorySize, (int)L_END);
  egnn_k1<<<NBK + K1NODEBLK + 1, K1T, 0, stream>>>(h, pos, (const u32x4*)d_in[2], (const u32x4*)d_in[3], (const u32x4*)d_in[4], we, wh, (char*)d_ws);
  egnn_k2<<<NBK, NW * 64, L_END, stream>>>(h, pos, wx, (const char*)d_ws, (u16*)d_out);
}
```

```cpp
#include <hip/hip_runtime.h>
#include <hip/hip_bf16.h>
#include <stdint.h>

typedef unsigned short u16;
typedef unsigned int u32;
typedef unsigned long long u64;
typedef __attribute__((ext_vector_type(16))) float f32x16;
typedef __attribute__((ext_vector_type(4))) float f32x4;
typedef __attribute__((ext_vector_type(2))) float f32x2;
typedef __attribute__((ext_vector_type(8))) __bf16 bf16x8;
typedef __attribute__((ext_vector_type(2))) __bf16 bf16x2;
typedef __attribute__((ext_vector_type(4))) short s16x4;
typedef __attribute__((ext_vector_type(4))) u32 u32x4;
typedef __attribute__((ext_vector_type(2))) u32 u32x2;
#define LAS __attribute__((address_space(3)))
#define GAS __attribute__((address_space(1)))
#define DI __device__ __forceinline__
#define RLX_AGENT __ATOMIC_RELAXED, __HIP_MEMORY_SCOPE_AGENT

constexpr int NN = 10000, NBK = 256, CH = 2500, CHS = 2560;
constexpr float NLOG2E = -1.4426950408889634f, NLN2 = -0.6931471805599453f;

#define WS_BASE 0
constexpr u32 WS_IDX = 0;
constexpr u32 WS_EA  = WS_IDX + NBK * CHS * 4;
constexpr u32 WS_TAB = WS_EA + NBK * CHS * 8;
constexpr u32 WS_BP  = WS_TAB + NBK * NBK * 8;
constexpr u32 WS_EP  = WS_BP + NN * 128;
constexpr u32 WS_END = WS_EP + 256;

#define STAMP_DECL
#define STAMP(i)
#define STAMP_OUT()

DI float b2f(u32 x) { return __uint_as_float(x << 16); }
DI float bhi(u32 x) { return __uint_as_float(x & 0xffff0000u); }
DI u32 pk2(float a, float b) { f32x2 v = {a, b}; return __builtin_bit_cast(u32, __builtin_convertvector(v, bf16x2)); }
DI float rbf(float a) { return b2f(pk2(a, 0.f) & 0xffffu); }
DI int first_node(int b) { return (b * 625 + 15) >> 4; }

template <int CTRL, int RM> DI u32 dpp0(u32 x) { return (u32)__builtin_amdgcn_update_dpp(0, (int)x, CTRL, RM, 0xf, false); }
DI u32 scan_add(u32 x) {
  x += dpp0<0x111, 0xf>(x); x += dpp0<0x112, 0xf>(x); x += dpp0<0x114, 0xf>(x); x += dpp0<0x118, 0xf>(x);
  x += dpp0<0x142, 0xa>(x); x += dpp0<0x143, 0xc>(x); return x;
}
DI u32 umax(u32 a, u32 b) { return a > b ? a : b; }
DI u32 umin(u32 a, u32 b) { return a < b ? a : b; }
DI u32 scan_max(u32 x) {
  x = umax(x, dpp0<0x111, 0xf>(x)); x = umax(x, dpp0<0x112, 0xf>(x)); x = umax(x, dpp0<0x114, 0xf>(x)); x = umax(x, dpp0<0x118, 0xf>(x));
  x = umax(x, dpp0<0x142, 0xa>(x)); x = umax(x, dpp0<0x143, 0xc>(x)); return x;
}
template <int CTRL> DI float dppf(float v) { return __uint_as_float((u32)__builtin_amdgcn_update_dpp(0, (int)__float_as_uint(v), CTRL, 0xf, 0xf, false)); }
DI u32x2 tr_read(u32 addr) { return __builtin_bit_cast(u32x2, __builtin_amdgcn_ds_read_tr16_b64_v4i16((LAS s16x4*)(size_t)addr)); }
template <typename T> DI T lds_ld(u32 addr) { return *(LAS T*)(size_t)addr; }
template <typename T> DI void lds_st(u32 addr, T v) { *(LAS T*)(size_t)addr = v; }
DI u32 lds_add(u32 addr, u32 v) { return __hip_atomic_fetch_add((LAS u32*)(size_t)addr, v, __ATOMIC_RELAXED, __HIP_MEMORY_SCOPE_WORKGROUP); }
DI int med3(int a, int lo, int hi) { int r; asm("v_med3_i32 %0, %1, %2, %3" : "=v"(r) : "v"(a), "v"(lo), "v"(hi)); return r; }
DI void dma16(const void* base, u32 voff, u32 ldsaddr) {
  asm volatile("s_mov_b32 m0, %2\n\ts_nop 0\n\tglobal_load_lds_dwordx4 %0, %1" ::"v"(voff), "s"(base), "s"(ldsaddr) : "memory");
}
DI void dma16_dev(const void* base, u32 voff, u32 ldsaddr) {
  asm volatile("s_mov_b32 m0, %2\n\ts_nop 0\n\tglobal_load_lds_dwordx4 %0, %1 sc1" ::"v"(voff), "s"(base), "s"(ldsaddr) : "memory");
}
DI void st16_dev(const void* base, u32 voff, u32x4 v) {
  asm volatile("global_store_dwordx4 %0, %1, %2 sc1\n\ts_nop 1" ::"v"(voff), "v"(v), "s"(base) : "memory");
}
#define WAITV(n) do { asm volatile("s_waitcnt vmcnt(" #n ")" ::: "memory"); __builtin_amdgcn_sched_barrier(0); } while (0)
DI void wg_barrier() { asm volatile("s_waitcnt lgkmcnt(0)\n\ts_barrier" ::: "memory"); }
DI bf16x8 as_frag(u32x4 v) { return __builtin_bit_cast(bf16x8, v); }
DI bf16x8 as_frag(u32x2 a, u32x2 b) { return __builtin_bit_cast(bf16x8, u32x4{a[0], a[1], b[0], b[1]}); }

constexpr int NW = 12, SW = (NBK + NW - 1) / NW, PASSES = 5, CAP = 2816, QSAFE = 192;
constexpr int STGS = 1088, STGW = 4 * STGS, MROW = 68;
constexpr u32 L_STG = 0;
constexpr u32 L_ZERO = L_STG + NW * STGW;
constexpr u32 L_REC = L_ZERO + 3328;
constexpr u32 L_DV = L_REC + CAP * 16;
constexpr u32 L_S = L_DV + (CAP + 48) * 8;
constexpr u32 L_M = L_S + (CAP + 64) * 4;
constexpr u32 L_P = L_M + 40 * MROW * 4;
constexpr u32 L_G = L_P + 40 * 48 * 4;
constexpr u32 L_OH = L_G + 56 * 128;
constexpr u32 L_CNT = L_OH + 128;
constexpr u32 L_MSK = L_CNT + 256;
constexpr u32 L_FLG = L_MSK + 256;
constexpr u32 L_END = L_FLG + 16;
constexpr u32 PART_P = 8 * MROW * 4;
constexpr u32 L_SIDX = 0, L_SEA = CHS * 4, L_HIST = CHS * 12, L_OFFS = L_HIST + 1024, L_W12 = L_OFFS + 1024;
constexpr u32 L_POS = L_REC, L_WH = L_POS + 60 * 1024;
constexpr u32 DSTR = 1280;
static_assert(L_OH % 128 == 0 && L_ZERO % 256 == 0 && L_END <= 163840, "lds layout");
static_assert(QSAFE * NW <= CAP && QSAFE <= PASSES * 64, "safe round size");
static_assert(L_W12 + 17 * 1024 <= L_ZERO && L_WH + 16384 <= L_M, "aliases");

DI void node_prod(int r0, int lane, u32x4 hf0, u32x4 hf1, u32x4& v0, u32x4& v1) {
  const int g = lane >> 4, q = (lane >> 2) & 3, p = lane & 3;
  u32 o[8];
#pragma unroll
  for (int T = 0; T < 4; ++T) {
    f32x4 acc = {0.f, 0.f, 0.f, 0.f};
#pragma unroll
    for (int ks = 0; ks < 2; ++ks) {
      const u32 a0 = L_W12 + (u32)((r0 + 32 * ks + 8 * g + q) * 128 + (16 * p + 4 * T) * 2);
      acc = __builtin_amdgcn_mfma_f32_16x16x32_bf16(as_frag(tr_read(a0), tr_read(a0 + 512)), as_frag(ks ? hf1 : hf0), acc, 0, 0, 0);
    }
    o[2 * T] = pk2(acc[0] * NLOG2E, acc[1] * NLOG2E);
    o[2 * T + 1] = pk2(acc[2] * NLOG2E, acc[3] * NLOG2E);
  }
  v0 = u32x4{o[0], o[1], o[2], o[3]}; v1 = u32x4{o[4], o[5], o[6], o[7]};
}

__global__ __launch_bounds__(NW * 64) void egnn_fused(const u16* h, const u16* pos, const u32x4* ea4, const u32x4* src4, const u32x4* dst4,
                                                      const u16* we, const u16* wx, const u16* wh, char* ws, u16* out) {
  STAMP_DECL
  STAMP(0);
  asm volatile("" : "+s"(h), "+s"(pos), "+s"(ea4), "+s"(src4), "+s"(dst4), "+s"(we), "+s"(wx), "+s"(wh), "+s"(ws), "+s"(out));
  const int tid = threadIdx.x;
  int lane = tid & 63;
  const int w = __builtin_amdgcn_readfirstlane(tid >> 6);
  const int c = blockIdx.x;
  const int b = (c & 7) * 32 + (c >> 3);
  const int n0 = first_node(b), nn = first_node(b + 1) - n0;
  const u32 stg = L_STG + (u32)w * STGW;

  const bool act = tid < CH / 4;
  const int etile = w % 3, eT = w / 3;
  const int enl = etile * 16 + (lane & 15);
  const int en = n0 + (enl < nn ? enl : 0);
  u32x4 d, s, e0, e1, hf0, hf1;
  u32x2 hr;
  u64 epoch;
  u32 wxr0, wxr1;
  {
    const u32 q = (u32)(c * (CH / 4) + (act ? tid : 0));
    const u32 ho = (u32)((n0 + (lane & 15)) * 128 + (lane >> 4) * 16);
    const void* pd = w < 10 ? (const void*)dst4 : (const void*)h;
    const void* ps = w < 10 ? (const void*)src4 : (const void*)(h + 32);
    const u32 o1 = w < 10 ? q * 16 : ho;
    asm volatile("global_load_dwordx4 %0, %4, %5 nt\n\tglobal_load_dwordx4 %1, %4, %6 nt\n\tglobal_load_dwordx4 %2, %7, %8 nt\n\tglobal_load_dwordx4 %3, %7, %8 offset:16 nt"
                 : "=&v"(d), "=&v"(s), "=&v"(e0), "=&v"(e1) : "v"(o1), "s"(pd), "s"(ps), "v"(q * 32), "s"(ea4) : "memory");
    asm volatile("global_load_dwordx4 %0, %6, %7\n\tglobal_load_dwordx4 %1, %6, %7 offset:64\n\tglobal_load_dwordx2 %2, %8, %7\n\t"
                 "global_load_dwordx2 %3, %9, %10 sc1\n\tglobal_load_ushort %4, %11, %12\n\tglobal_load_ushort %5, %11, %12 offset:64"
                 : "=&v"(hf0), "=&v"(hf1), "=&v"(hr), "=&v"(epoch), "=&v"(wxr0), "=&v"(wxr1)
                 : "v"((u32)(en * 128 + (lane >> 4) * 16)), "s"(h), "v"((u32)(en * 128 + eT * 32 + (lane >> 4) * 8)), "v"(WS_EP), "s"(ws),
                   "v"((u32)((lane & 31) * 2)), "s"(wx) : "memory");
#pragma unroll 1
    for (int i = 0; i < 8; ++i) {
      const int j = w * 8 + i;
      const int pc = j < 80 ? j - 76 : (i < 4 ? 4 * w - 32 + i : (i < 6 ? 2 * w - 20 + i : 16));
      const void* base = j < 60 ? (const void*)pos : (j < 76 ? (const void*)wh : (const void*)we);
      const u32 off = (u32)(j < 60 ? j : (j < 76 ? j - 60 : pc)) * 1024;
      const u32 lim = j < 60 ? NN * 6 - 16 : 133 * 128 - 16;
      dma16(base, umin(off + lane * 16, lim), j < 76 ? L_POS + (u32)j * 1024 : L_W12 + off);
    }
    const u32x4 z4 = {0, 0, 0, 0};
    if (tid < 64) lds_st<u32x4>(L_HIST + tid * 16, z4);
    else if (tid < 64 + 3328 / 16) lds_st<u32x4>(L_ZERO + (tid - 64) * 16, z4);
    else if (tid < 320) { if (tid < 272 + 33) lds_st<u32x4>(L_CNT + (tid - 272) * 16, z4); }
    else if (tid < 352) lds_st<u32>(L_OH + (tid - 320) * 4, (((tid >> 3) & 3) == (tid & 3)) ? (((tid >> 2) & 1) ? 0x3F800000u : 0x3F80u) : 0u);
    lds_st<u32x4>(L_M + tid * 16, z4);
    if (tid < (L_G - L_M) / 16 - NW * 64) lds_st<u32x4>(L_M + (NW * 64 + tid) * 16, z4);
  }
  wg_barrier();
  STAMP(1);
  if (w < 10) {
    u32 bk[4], rk[4];
    WAITV(17); asm volatile("" : "+v"(d));
#pragma unroll
    for (int k = 0; k < 4; ++k) bk[k] = (d[k] * 16u) / 625u;
#pragma unroll
    for (int k = 0; k < 4; ++k) rk[k] = lds_add(L_HIST + bk[k] * 4, act ? 1u : 0u);
    STAMP(2);
    wg_barrier();
    const u32x4 cn = lds_ld<u32x4>(L_HIST + lane * 16);
    const u32 tot = cn[0] + cn[1] + cn[2] + cn[3];
    const u32 ex = scan_add(tot) - tot;
    lds_st<u32x4>(L_OFFS + lane * 16, u32x4{ex, ex + cn[0], ex + cn[0] + cn[1], ex + cn[0] + cn[1] + cn[2]});
    asm volatile("" ::: "memory");
#pragma unroll
    for (int k = 0; k < 4; ++k) rk[k] = umin(rk[k] + lds_ld<u32>(L_OFFS + bk[k] * 4) + (act ? 0u : 65536u), CH + 4);
    WAITV(16); asm volatile("" : "+v"(s));
#pragma unroll
    for (int k = 0; k < 4; ++k) lds_st<u32>(L_SIDX + rk[k] * 4, (s[k] << 7) | (d[k] - (u32)first_node(bk[k])));
    WAITV(15); asm volatile("" : "+v"(e0));
    lds_st<u32x2>(L_SEA + rk[0] * 8, u32x2{e0[0], e0[1]}); lds_st<u32x2>(L_SEA + rk[1] * 8, u32x2{e0[2], e0[3]});
    WAITV(14); asm volatile("" : "+v"(e1));
    lds_st<u32x2>(L_SEA + rk[2] * 8, u32x2{e1[0], e1[1]}); lds_st<u32x2>(L_SEA + rk[3] * 8, u32x2{e1[2], e1[3]});
    WAITV(0); asm volatile("" : "+v"(hf0), "+v"(hf1), "+v"(hr), "+v"(epoch), "+v"(wxr0), "+v"(wxr1));
  } else {
    WAITV(4); asm volatile("" : "+v"(d), "+v"(s), "+v"(e0), "+v"(e1), "+v"(hf0), "+v"(hf1), "+v"(hr), "+v"(epoch), "+v"(wxr0), "+v"(wxr1));
    STAMP(2);
    wg_barrier();
    u32x4 v0, v1;
    node_prod(64, lane, hf0, hf1, v0, v1);
    if (enl < nn) { const u32 o2 = WS_BP + (u32)en * 128 + (lane >> 4) * 32; st16_dev(ws, o2, v0); st16_dev(ws, o2 + 16, v1); }
    if (w == 10) {
      node_prod(64, lane, d, s, v0, v1);
      const u32 o = WS_BP + (u32)(n0 + (lane & 15)) * 128 + (lane >> 4) * 32;
      st16_dev(ws, o, v0); st16_dev(ws, o + 16, v1);
      asm volatile("s_waitcnt vmcnt(4)" ::: "memory");
    } else {
      asm volatile("s_waitcnt vmcnt(2)" ::: "memory");
    }
  }
  STAMP(3);
  wg_barrier();
  STAMP(4);
  if (act) {
    const u32x4 a = lds_ld<u32x4>(L_SIDX + tid * 16), x = lds_ld<u32x4>(L_SEA + tid * 16), y = lds_ld<u32x4>(L_SEA + 10000 + tid * 16);
    st16_dev(ws, WS_IDX + (u32)c * CHS * 4 + tid * 16, a);
    st16_dev(ws, WS_EA + (u32)c * CHS * 8 + tid * 16, x);
    st16_dev(ws, WS_EA + (u32)c * CHS * 8 + 10000 + tid * 16, y);
  }
  if (w >= 9) {
    u32x4 v0, v1;
    node_prod(0, lane, hf0, hf1, v0, v1);
    lds_st<u32x4>(L_G + enl * 128 + (lane >> 4) * 32, v0); lds_st<u32x4>(L_G + enl * 128 + (lane >> 4) * 32 + 16, v1);
  }
  if (tid < 512) {
    const int r = tid >> 6;
    const float x = b2f(lds_ld<u16>(L_W12 + (r < 4 ? 129 + r : 128) * 128 + lane * 2)) * NLOG2E;
    const float y = r < 5 ? x : (r == 5 ? x - rbf(x) : 0.f);
    lds_st<u16>(L_G + (48 + r) * 128 + lane * 2, (u16)(pk2(y, 0.f) & 0xffffu));
  }
  f32x4 z = {0.f, 0.f, 0.f, 0.f};
  bf16x8 wh2[2];
  {
    const u32 a0 = L_WH + (u32)((8 * (lane >> 4) + ((lane >> 2) & 3)) * 128 + (16 * eT + 4 * (lane & 3)) * 2);
    z = __builtin_amdgcn_mfma_f32_16x16x32_bf16(as_frag(tr_read(a0), tr_read(a0 + 512)), as_frag(hf0), z, 0, 0, 0);
    z = __builtin_amdgcn_mfma_f32_16x16x32_bf16(as_frag(tr_read(a0 + 4096), tr_read(a0 + 4096 + 512)), as_frag(hf1), z, 0, 0, 0);
    wh2[0] = as_frag(tr_read(a0 + 8192), tr_read(a0 + 8192 + 512));
    wh2[1] = as_frag(tr_read(a0 + 12288), tr_read(a0 + 12288 + 512));
  }
  const u32 pold = lds_ld<u16>(L_POS + (u32)(n0 * 3 + ((tid >> 2) < nn * 3 ? (tid >> 2) : 0)) * 2);
  const float wx0 = b2f(wxr0) * NLN2, wx1 = b2f(wxr1) * NLN2;
  const u64 tag = (epoch + 1) << 24;
  u32 myte = 0;
  if (tid < 256) myte = lds_ld<u32>(L_OFFS + tid * 4) | (lds_ld<u32>(L_HIST + tid * 4) << 12);
  asm volatile("s_waitcnt vmcnt(0)" ::: "memory");
  STAMP(5);
  wg_barrier();
  if (tid < 256) __hip_atomic_store((GAS u64*)(ws + WS_TAB) + c * NBK + tid, tag | myte, RLX_AGENT);
  const u32 dsc = (u32)w * DSTR;
  lds_st<u32x4>(dsc + lane * 16, u32x4{0, 0, 0, 0});
  if (lane < 16) lds_st<u32x4>(dsc + 1024 + lane * 16, u32x4{0, 0, 0, 0});

  const int seg = w * SW + lane;
  const bool sv = lane < SW && seg < NBK;
  u32 te = 0;
  {
    bool ok = !sv;
#pragma unroll 1
    for (u32 spins = 0; spins < (1u << 22); ++spins) {
      if (!ok) {
        const u64 g = __hip_atomic_load((GAS u64*)(ws + WS_TAB) + seg * NBK + b, RLX_AGENT);
        ok = (g >> 24) == (tag >> 24);
        te = ok ? (u32)g & 0xffffffu : 0u;
      }
      if (__all(ok)) break;
      __builtin_amdgcn_s_sleep(2);
    }
    __builtin_amdgcn_fence(__ATOMIC_ACQUIRE, "wavefront");
  }
  STAMP(6);
  const u32 scnt = te >> 12;
  const u32 sincl = scan_add(scnt);
  const u32 sexcl = sincl - scnt;
  const u32 Tw = __builtin_amdgcn_readlane(sincl, 63);
  const u32 delta = (u32)seg * CHS + (te & 0xfffu) - sexcl;

  u32 Q = PASSES * 64, vbase = 0;
  bool firstpass = true;
  for (;;) {
    asm volatile("" : "+v"(lane));
    if (scnt > 0 && sexcl < vbase + Q) {
      const u32 pp = sexcl > vbase ? sexcl - vbase : 0u;
      __hip_atomic_fetch_max((LAS u32*)(size_t)(dsc + pp * 4), delta, __ATOMIC_RELAXED, __HIP_MEMORY_SCOPE_WAVEFRONT);
    }
    asm volatile("" ::: "memory");
    const u32 vend = umin(Tw, vbase + Q);
    u32 ridx[PASSES], rrank[PASSES], rd2[PASSES];
    u32x2 rea[PASSES], rdv[PASSES];
    {
      u32 ri[PASSES], carry = 0;
#pragma unroll
      for (int k = 0; k < PASSES; ++k) ri[k] = scan_max(lds_ld<u32>(dsc + (64 * k + lane) * 4));
#pragma unroll
      for (int k = 0; k < PASSES; ++k) {
        const u32 dl = umax(ri[k], carry);
        carry = __builtin_amdgcn_readlane(dl, 63);
        const u32 v = vbase + 64 * k + lane;
        ri[k] = v < vend ? v + dl : (u32)c * CHS;
        ridx[k] = __hip_atomic_load((GAS u32*)(ws + WS_IDX) + ri[k], RLX_AGENT);
      }
#pragma unroll
      for (int k = 0; k < PASSES; ++k) rea[k] = __builtin_bit_cast(u32x2, __hip_atomic_load((GAS u64*)(ws + WS_EA) + ri[k], RLX_AGENT));
    }
    STAMP(7);
    if (firstpass && Tw > Q && lane == 0) lds_st<u32>(L_FLG, 1u);
    {
      u32x2 ps[PASSES], pd[PASSES];
#pragma unroll
      for (int k = 0; k < PASSES; ++k) {
        const bool on = vbase + 64 * k + lane < vend;
        const u32 nl = ridx[k] & 63u, sn = ridx[k] >> 7, dn = umin(n0 + nl, NN - 1);
        rrank[k] = lds_add(L_CNT + (on ? nl : 63u) * 4, on ? 1u : 0u);
        const u32 sa = L_POS + ((sn * 3) >> 1) * 4, da = L_POS + ((dn * 3) >> 1) * 4;
        ps[k] = u32x2{lds_ld<u32>(sa), lds_ld<u32>(sa + 4)};
        pd[k] = u32x2{lds_ld<u32>(da), lds_ld<u32>(da + 4)};
      }
#pragma unroll
      for (int k = 0; k < PASSES; ++k) {
        const u32 nl = ridx[k] & 63u, sn = ridx[k] >> 7, dn = umin(n0 + nl, NN - 1);
        const u64 a = __builtin_bit_cast(u64, ps[k]) >> ((sn & 1) * 16), bb = __builtin_bit_cast(u64, pd[k]) >> ((dn & 1) * 16);
        const u32 dxy = pk2(b2f((u32)bb) - b2f((u32)a), bhi((u32)bb) - bhi((u32)a));
        const u32 dz = pk2(b2f((u32)(bb >> 32)) - b2f((u32)(a >> 32)), 0.f);
        const float x = b2f(dxy), y = bhi(dxy), zz = b2f(dz);
        const u32 sxy = pk2(x * x, y * y), sz = pk2(zz * zz, 0.f);
        const float d2 = b2f(sxy) + bhi(sxy) + b2f(sz);
        rd2[k] = pk2(d2, d2);
        rdv[k][0] = 0x3F80u | (dxy << 16);
        rdv[k][1] = (dxy >> 16) | (dz << 16);
        asm volatile("" : "+v"(rd2[k]), "+v"(rdv[k]));
      }
    }
    STAMP(8);
    wg_barrier();
    STAMP(9);
    const u32 cn = lds_ld<u32>(L_CNT + lane * 4);
    const u32 nincl = scan_add(cn);
    const u32 nstart = nincl - cn;
    const u32 total = __builtin_amdgcn_readlane(nincl, 63);
    if (firstpass) {
      if (c == 0 && tid == 0) __hip_atomic_store((GAS u64*)(ws + WS_EP), epoch + 1, RLX_AGENT);
      const u32 flg = __builtin_amdgcn_readfirstlane(lds_ld<u32>(L_FLG));
      firstpass = false;
      if (flg || total > (u32)CAP) {
        wg_barrier();
        if (w == 0) { lds_st<u32>(L_CNT + lane * 4, 0u); lds_st<u32>(L_FLG, 0u); }
        lds_st<u32x4>(dsc + lane * 16, u32x4{0, 0, 0, 0});
        wg_barrier();
        if (lane == 0) __hip_atomic_fetch_max((LAS u32*)(size_t)L_FLG, Tw, __ATOMIC_RELAXED, __HIP_MEMORY_SCOPE_WORKGROUP);
        Q = QSAFE;
        continue;
      }
    }
#pragma unroll
    for (int k = 0; k < PASSES; ++k) rrank[k] += (u32)__builtin_amdgcn_ds_bpermute((int)((ridx[k] & 63u) * 4), (int)nstart);
#pragma unroll
    for (int k = 0; k < PASSES; ++k) {
      if (vbase + 64 * k + lane < vend) {
        const u32 nl = ridx[k] & 63u, slot = rrank[k];
        lds_st<u32>(L_S + slot * 4, (ridx[k] & ~127u) | ((nl & 7u) << 4));
        lds_st<u32x2>(L_DV + (slot + nl + 1) * 8, rdv[k]);
        lds_st<u32x4>(L_REC + slot * 16, u32x4{rea[k][0], rea[k][1], rd2[k], 0u});
      }
    }
    if (w == NW - 1) { if (lane <= 41) lds_st<u32x2>(L_DV + (nstart + lane) * 8, u32x2{0, 0}); }
    if (w == NW - 1) { lds_st<u32>(L_S + (total + lane) * 4, 0u); if (lane < 32 && total + lane < (u32)CAP) lds_st<u32x4>(L_REC + (total + lane) * 16, u32x4{0, 0, 0, 0}); }
    STAMP(10);
    wg_barrier();
    STAMP(11);

    asm volatile("" : "+v"(lane));
    const char* Bp = ws + WS_BP;
    const int ntiles = (int)(total + 31) >> 5;
    const int tb = (ntiles * w) / NW, tend = (ntiles * (w + 1)) / NW;
    const int hh = lane >> 5, blk = (lane >> 4) & 1, q = (lane >> 2) & 3, p = lane & 3;
    const u32 cbase = (q & 1) ? stg + 128 * hh + 32 * blk + 8 * p + (q >> 1) * STGS : L_ZERO + 128 * (1 - hh);
    const u32 maskh = hh ? 0x70u : 0u, fstep = hh ? 0u : 512u;
    const u32 lanepart = (lane & 7) * 16;
    u32 sbeg = (u32)tb * 32;
    const u32 send = umin((u32)tend * 32, total);
    while (sbeg < send) {
      const int t0 = (int)(sbeg >> 5);
      u32 s4a = L_S + (u32)(t0 * 32 + 4 * (lane >> 3)) * 4, s1a = L_S + (u32)(t0 * 32 + (lane & 31)) * 4;
      {
        const u32x4 s4 = lds_ld<u32x4>(s4a);
#pragma unroll
        for (int i = 0; i < 4; ++i) dma16_dev(Bp, (s4[i] & ~127u) | lanepart, stg + i * STGS);
      }
      u32 s1 = lds_ld<u32>(s1a);
      const int base = __builtin_popcountll(__ballot(nstart <= sbeg && lane < nn)) - 1;
      const int bend = base + 8 < nn ? base + 8 : nn;
      const u32 wend = umin(__builtin_amdgcn_readlane(nstart, bend), send);
      const int t1 = (int)((wend + 31) >> 5);
      bf16x8 wf[2];
      {
        const int r0 = hh ? base + ((q - base) & 7) : 48 + q, r1 = hh ? base + ((4 + q - base) & 7) : 52 + q;
        const u32 a0 = L_G + r0 * 128 + 32 * blk + 8 * p, a1 = L_G + r1 * 128 + 32 * blk + 8 * p;
        wf[0] = as_frag(tr_read(a0), tr_read(a1));
        wf[1] = as_frag(tr_read(a0 + 64), tr_read(a1 + 64));
      }
      int np = base + ((4 * blk + p - base) & 7); np = np < nn ? np : nn;
      const int lo = (int)L_DV + 8 * ((int)__builtin_amdgcn_ds_bpermute(np * 4, (int)nstart) + np);
      const int hi = (int)L_DV + 8 * ((int)__builtin_amdgcn_ds_bpermute(np * 4 + 4, (int)nstart) + np + 1);
      int da = (int)L_DV + 8 * (t0 * 32 + 4 * hh + q + np + 1);
      u32 fb = hh ? L_OH : L_REC + (u32)(t0 * 32 + (lane & 31)) * 16;
      f32x16 acc0, acc1;
#pragma unroll
      for (int i = 0; i < 16; ++i) { acc0[i] = 0.f; acc1[i] = 0.f; }
      int rem = t1 - t0;
      do {
        __builtin_amdgcn_s_setprio(3);
        asm volatile("s_waitcnt vmcnt(0)" ::: "memory");
        f32x16 x0, x1;
        const u32x4 ff = lds_ld<u32x4>((s1 & maskh) | fb);
#pragma unroll
        for (int k = 0; k < 8; ++k) {
          const u32x2 a = tr_read(cbase + 2 * STGS * (k & 1) + 256 * (k >> 1));
          x0[2 * k] = __uint_as_float(a[0]); x0[2 * k + 1] = __uint_as_float(a[1]);
        }
#pragma unroll
        for (int k = 0; k < 8; ++k) {
          const u32x2 a = tr_read(cbase + 2 * STGS * (k & 1) + 256 * (k >> 1) + 64);
          x1[2 * k] = __uint_as_float(a[0]); x1[2 * k + 1] = __uint_as_float(a[1]);
        }
        s4a += 128; s1a += 128;
        const u32x4 s4 = lds_ld<u32x4>(s4a);
        u32x2 a2[4];
#pragma unroll
        for (int i = 0; i < 4; ++i) a2[i] = tr_read((u32)med3(da, lo - 64 * i, hi - 64 * i) + 64 * i);
        s1 = lds_ld<u32>(s1a);
        fb += fstep; da += 256;
        __builtin_amdgcn_sched_barrier(0);
        x0 = __builtin_amdgcn_mfma_f32_32x32x16_bf16(as_frag(ff), wf[0], x0, 0, 0, 0);
        x1 = __builtin_amdgcn_mfma_f32_32x32x16_bf16(as_frag(ff), wf[1], x1, 0, 0, 0);
        --rem;
        if (rem > 0) {
#pragma unroll
          for (int i = 0; i < 4; ++i) dma16_dev(Bp, (s4[i] & ~127u) | lanepart, stg + i * STGS);
        }
        if (rem == 0) __builtin_amdgcn_s_setprio(0);
        else if (rem == 1) __builtin_amdgcn_s_setprio(1);
        else __builtin_amdgcn_s_setprio(2);
        __builtin_amdgcn_sched_barrier(0);
        const bf16x8 sa0 = as_frag(a2[0], a2[1]), sa1 = as_frag(a2[2], a2[3]);
        u32 m[8];
        f32x2 e[8];
#pragma unroll
        for (int hf = 0; hf < 2; ++hf) {
          const f32x16& x = hf ? x1 : x0;
#pragma unroll
          for (int i = 0; i < 8; ++i) {
            e[i][0] = __builtin_amdgcn_exp2f(x[2 * i]); e[i][1] = __builtin_amdgcn_exp2f(x[2 * i + 1]);
            if (hf == 1 && i == 0) { __builtin_amdgcn_sched_barrier(0); acc0 = __builtin_amdgcn_mfma_f32_32x32x16_bf16(sa0, as_frag(u32x4{m[0], m[1], m[2], m[3]}), acc0, 0, 0, 0); __builtin_amdgcn_sched_barrier(0); }
            if (hf == 1 && i == 2) { __builtin_amdgcn_sched_barrier(0); acc0 = __builtin_amdgcn_mfma_f32_32x32x16_bf16(sa1, as_frag(u32x4{m[4], m[5], m[6], m[7]}), acc0, 0, 0, 0); __builtin_amdgcn_sched_barrier(0); }
          }
          __builtin_amdgcn_sched_barrier(0);
#pragma unroll
          for (int i = 0; i < 8; ++i) e[i] += 1.0f;
          __builtin_amdgcn_sched_barrier(0);
#pragma unroll
          for (int i = 0; i < 8; ++i) { e[i][0] = __builtin_amdgcn_rcpf(e[i][0]); e[i][1] = __builtin_amdgcn_rcpf(e[i][1]); }
          __builtin_amdgcn_sched_barrier(0);
#pragma unroll
          for (int i = 0; i < 8; ++i) { const f32x2 xv = {x[2 * i], x[2 * i + 1]}; e[i] *= xv; }
          __builtin_amdgcn_sched_barrier(0);
#pragma unroll
          for (int i = 0; i < 8; ++i) m[i] = pk2(e[i][0], e[i][1]);
          __builtin_amdgcn_sched_barrier(0);
        }
        acc1 = __builtin_amdgcn_mfma_f32_32x32x16_bf16(sa0, as_frag(u32x4{m[0], m[1], m[2], m[3]}), acc1, 0, 0, 0);
        acc1 = __builtin_amdgcn_mfma_f32_32x32x16_bf16(sa1, as_frag(u32x4{m[4], m[5], m[6], m[7]}), acc1, 0, 0, 0);
      } while (rem > 0);
      STAMP(12);
      asm volatile("" : "+v"(lane));
      const int lastn = __builtin_popcountll(__ballot(nstart < wend && lane < nn)) - 1;
      const bool priv = wend >= send && Q == PASSES * 64;
      const int h2 = lane >> 5;
      if (priv) {
        if (lane >= base && lane <= lastn)
          __hip_atomic_fetch_or((LAS u32*)(size_t)(L_MSK + lane * 4), 1u << w, __ATOMIC_RELAXED, __HIP_MEMORY_SCOPE_WORKGROUP);
        const u32 ma = stg + (u32)(h2 * MROW + (lane & 31)) * 4, pa = stg + PART_P + (u32)(h2 * 48 + ((lane & 31) >> 1)) * 4;
#pragma unroll
        for (int g4 = 0; g4 < 4; ++g4) {
          lds_st<float>(ma + g4 * 2 * MROW * 4, acc0[4 * g4]);
          lds_st<float>(ma + g4 * 2 * MROW * 4 + 128, acc1[4 * g4]);
#pragma unroll
          for (int cc = 0; cc < 3; ++cc) {
            const float v = acc0[4 * g4 + 1 + cc] * wx0 + acc1[4 * g4 + 1 + cc] * wx1;
            lds_st<float>(pa + g4 * 384 + 64 * cc, v + dppf<0xB1>(v));
          }
        }
      } else {
#pragma unroll
        for (int g4 = 0; g4 < 4; ++g4) {
          float pv[3];
#pragma unroll
          for (int cc = 0; cc < 3; ++cc) {
            const float v = acc0[4 * g4 + 1 + cc] * wx0 + acc1[4 * g4 + 1 + cc] * wx1;
            pv[cc] = v + dppf<0xB1>(v);
          }
          const int nd = base + ((2 * g4 + h2 - base) & 7);
          if (nd <= lastn) {
            const u32 ma = L_M + (u32)(nd * MROW + (lane & 31)) * 4;
            __hip_atomic_fetch_add((LAS float*)(size_t)ma, acc0[4 * g4], __ATOMIC_RELAXED, __HIP_MEMORY_SCOPE_WORKGROUP);
            __hip_atomic_fetch_add((LAS float*)(size_t)(ma + 128), acc1[4 * g4], __ATOMIC_RELAXED, __HIP_MEMORY_SCOPE_WORKGROUP);
            if (!(lane & 1)) {
              const u32 pa = L_P + (u32)(nd * 48 + ((lane & 31) >> 1)) * 4;
#pragma unroll
              for (int cc = 0; cc < 3; ++cc) __hip_atomic_fetch_add((LAS float*)(size_t)(pa + 64 * cc), pv[cc], __ATOMIC_RELAXED, __HIP_MEMORY_SCOPE_WORKGROUP);
            }
          }
        }
      }
      sbeg = wend;
    }
    if (Q == PASSES * 64) break;
    vbase += Q;
    const u32 tmax = __builtin_amdgcn_readfirstlane(lds_ld<u32>(L_FLG));
    if (vbase >= tmax) break;
    wg_barrier();
    asm volatile("" : "+v"(lane));
    if (w == 0) lds_st<u32>(L_CNT + lane * 4, 0u);
    lds_st<u32x4>(dsc + lane * 16, u32x4{0, 0, 0, 0});
#pragma unroll
    for (int i = 0; i < 5; ++i) dma16(pos, umin((u32)((w * 5 + i) * 1024 + lane * 16), (u32)(NN * 6 - 16)), L_POS + (u32)(w * 5 + i) * 1024);
    asm volatile("s_waitcnt vmcnt(0)" ::: "memory");
    wg_barrier();
  }

  STAMP(13);
  wg_barrier();
  STAMP(14);
  {
    asm volatile("" : "+v"(lane));
    const int t2 = w * 64 + lane, g = lane >> 4;
    const int nl = etile * 16 + (lane & 15);
    const bool nv = nl < nn;
    const int pi = t2 >> 2;
    const bool pvld = pi < nn * 3;
    const int pn = pvld ? pi / 3 : 0, pc = pi - pn * 3;
    u32 mk = lds_ld<u32>(L_MSK + (nv ? nl : 63) * 4), pk = lds_ld<u32>(L_MSK + (pvld ? pn : 63) * 4);
    f32x4 mm[4], pp;
    {
      const u32 ma = L_M + (u32)((nv ? nl : 0) * MROW + 8 * g) * 4;
      mm[0] = lds_ld<f32x4>(ma); mm[1] = lds_ld<f32x4>(ma + 16); mm[2] = lds_ld<f32x4>(ma + 128); mm[3] = lds_ld<f32x4>(ma + 144);
      pp = lds_ld<f32x4>(L_P + (u32)((pvld ? pi : 0) * 16 + (lane & 3) * 4) * 4);
    }
    const u32 mo = (u32)((nl & 7) * MROW + 8 * g) * 4, po = PART_P + (u32)((pn & 7) * 48 + (pvld ? pc : 0) * 16 + (lane & 3) * 4) * 4;
    do {
#pragma unroll
      for (int u = 0; u < 2; ++u) {
        const u32 ma = mk ? L_STG + (u32)__builtin_ctz(mk) * STGW + mo : L_ZERO;
        const u32 pa = pk ? L_STG + (u32)__builtin_ctz(pk) * STGW + po : L_ZERO;
        mk &= mk - 1; pk &= pk - 1;
        mm[0] += lds_ld<f32x4>(ma); mm[1] += lds_ld<f32x4>(ma + 16); mm[2] += lds_ld<f32x4>(ma + 128); mm[3] += lds_ld<f32x4>(ma + 144);
        pp += lds_ld<f32x4>(pa);
      }
    } while (__ballot(mk | pk));
#pragma unroll
    for (int ks = 0; ks < 2; ++ks) {
      const f32x4 a = mm[2 * ks], cc = mm[2 * ks + 1];
      const u32x4 mf = {pk2(a[0] * NLN2, a[1] * NLN2), pk2(a[2] * NLN2, a[3] * NLN2), pk2(cc[0] * NLN2, cc[1] * NLN2), pk2(cc[2] * NLN2, cc[3] * NLN2)};
      z = __builtin_amdgcn_mfma_f32_16x16x32_bf16(wh2[ks], as_frag(mf), z, 0, 0, 0);
    }
    float o[4];
#pragma unroll
    for (int i = 0; i < 4; ++i) o[i] = z[i] * __builtin_amdgcn_rcpf(1.f + __builtin_amdgcn_exp2f(z[i] * NLOG2E));
    if (nv) {
      const u32x2 ov = {pk2(b2f(hr[0]) + o[0], bhi(hr[0]) + o[1]), pk2(b2f(hr[1]) + o[2], bhi(hr[1]) + o[3])};
      *(u32x2*)(out + (n0 + nl) * 64 + 16 * eT + 4 * g) = ov;
    }
    float ps = (pp[0] + pp[1]) + (pp[2] + pp[3]);
    ps += dppf<0xB1>(ps);
    ps += dppf<0x4E>(ps);
    if (pvld && (lane & 3) == 0) out[NN * 64 + n0 * 3 + pi] = (u16)(pk2(b2f(pold) + ps, 0.f) & 0xffffu);
  }
}

extern "C" void kernel_launch(void* const* d_in, const int* in_sizes, int n_in, void* d_out, int out_size, void* d_ws, size_t ws_size,
                              hipStream_t stream) {
  (void)hipFuncSetAttribute((const void*)egnn_fused, hipFuncAttributeMaxDynamicSharedMemorySize, (int)L_END);
  egnn_fused<<<NBK, NW * 64, L_END, stream>>>((const u16*)d_in[0], (const u16*)d_in[1], (const u32x4*)d_in[2], (const u32x4*)d_in[3],
                                              (const u32x4*)d_in[4], (const u16*)d_in[5], (const u16*)d_in[6], (const u16*)d_in[7],
                                              (char*)d_ws + WS_BASE, (u16*)d_out);
}
```

```cpp
#include <hip/hip_runtime.h>
#include <hip/hip_bf16.h>
#include <stdint.h>

typedef unsigned short u16;
typedef unsigned int u32;
typedef unsigned long long u64;
typedef __attribute__((ext_vector_type(16))) float f32x16;
typedef __attribute__((ext_vector_type(4))) float f32x4;
typedef __attribute__((ext_vector_type(2))) float f32x2;
typedef __attribute__((ext_vector_type(8))) __bf16 bf16x8;
typedef __attribute__((ext_vector_type(2))) __bf16 bf16x2;
typedef __attribute__((ext_vector_type(4))) short s16x4;
typedef __attribute__((ext_vector_type(4))) u32 u32x4;
typedef __attribute__((ext_vector_type(2))) u32 u32x2;
typedef __attribute__((ext_vector_type(8))) u64 u64x8;
typedef __attribute__((ext_vector_type(2))) u64 u64x2;
#define LAS __attribute__((address_space(3)))
#define GAS __attribute__((address_space(1)))
#define DI __device__ __forceinline__
#define RLX_AGENT __ATOMIC_RELAXED, __HIP_MEMORY_SCOPE_AGENT

constexpr int NN = 10000, NBK = 256, CH = 2500, CHS = 2560;
constexpr float NLOG2E = -1.4426950408889634f, NLN2 = -0.6931471805599453f;

#define WS_BASE 0
constexpr u32 WS_IDX = 0;
constexpr u32 WS_EA  = WS_IDX + NBK * CHS * 4;
constexpr u32 WS_TAB = WS_EA + NBK * CHS * 8;
constexpr u32 WS_FLE = WS_TAB + NBK * NBK * 8;
constexpr u32 WS_BP  = WS_FLE + NBK * 1024;
constexpr u32 WS_EP  = WS_BP + NN * 128;
constexpr u32 WS_ACC = WS_EP + 256;
constexpr u32 ACCS = 40 * 112 * 4;
constexpr u32 WS_END = WS_ACC + NBK * ACCS;

#define STAMP_DECL
#define STAMP(i)
#define STAMP_OUT()
#define CYC(i)

DI float b2f(u32 x) { return __uint_as_float(x << 16); }
DI float bhi(u32 x) { return __uint_as_float(x & 0xffff0000u); }
DI u32 pk2(float a, float b) { f32x2 v = {a, b}; return __builtin_bit_cast(u32, __builtin_convertvector(v, bf16x2)); }
DI float rbf(float a) { return b2f(pk2(a, 0.f) & 0xffffu); }
DI int first_node(int b) { return (b * 625 + 15) >> 4; }

template <int CTRL, int RM> DI u32 dpp0(u32 x) { return (u32)__builtin_amdgcn_update_dpp(0, (int)x, CTRL, RM, 0xf, false); }
DI u32 scan_add(u32 x) {
  x += dpp0<0x111, 0xf>(x); x += dpp0<0x112, 0xf>(x); x += dpp0<0x114, 0xf>(x); x += dpp0<0x118, 0xf>(x);
  x += dpp0<0x142, 0xa>(x); x += dpp0<0x143, 0xc>(x); return x;
}
DI u32 umax(u32 a, u32 b) { return a > b ? a : b; }
DI u32 umin(u32 a, u32 b) { return a < b ? a : b; }
DI u32 scan_max(u32 x) {
  x = umax(x, dpp0<0x111, 0xf>(x)); x = umax(x, dpp0<0x112, 0xf>(x)); x = umax(x, dpp0<0x114, 0xf>(x)); x = umax(x, dpp0<0x118, 0xf>(x));
  x = umax(x, dpp0<0x142, 0xa>(x)); x = umax(x, dpp0<0x143, 0xc>(x)); return x;
}
template <int CTRL> DI float dppf(float v) { return __uint_as_float((u32)__builtin_amdgcn_update_dpp(0, (int)__float_as_uint(v), CTRL, 0xf, 0xf, false)); }
DI u32x2 tr_read(u32 addr) { return __builtin_bit_cast(u32x2, __builtin_amdgcn_ds_read_tr16_b64_v4i16((LAS s16x4*)(size_t)addr)); }
template <typename T> DI T lds_ld(u32 addr) { return *(LAS T*)(size_t)addr; }
template <typename T> DI void lds_st(u32 addr, T v) { *(LAS T*)(size_t)addr = v; }
DI u32 lds_add(u32 addr, u32 v) { return __hip_atomic_fetch_add((LAS u32*)(size_t)addr, v, __ATOMIC_RELAXED, __HIP_MEMORY_SCOPE_WORKGROUP); }
DI int med3(int a, int lo, int hi) { int r; asm("v_med3_i32 %0, %1, %2, %3" : "=v"(r) : "v"(a), "v"(lo), "v"(hi)); return r; }
DI void dma16(const GAS void* base, u32 voff, u32 ldsaddr) {
  asm volatile("s_mov_b32 m0, %2\n\ts_nop 0\n\tglobal_load_lds_dwordx4 %0, %1" ::"v"(voff), "s"(base), "s"(ldsaddr) : "memory");
}
DI void dma16_dev(const GAS void* base, u32 voff, u32 ldsaddr) {
  asm volatile("s_mov_b32 m0, %2\n\ts_nop 0\n\tglobal_load_lds_dwordx4 %0, %1 sc1" ::"v"(voff), "s"(base), "s"(ldsaddr) : "memory");
}
DI void st16_dev(const GAS void* base, u32 voff, u32x4 v) {
  asm volatile("global_store_dwordx4 %0, %1, %2 sc1\n\ts_nop 1" ::"v"(voff), "v"(v), "s"(base) : "memory");
}
#define WAITV(n) WAITV_(n)
#define WAITV_(n) do { asm volatile("s_waitcnt vmcnt(" #n ")" ::: "memory"); __builtin_amdgcn_sched_barrier(0); } while (0)
DI void wg_barrier() { asm volatile("s_waitcnt lgkmcnt(0)\n\ts_barrier" ::: "memory"); }
DI bf16x8 as_frag(u32x4 v) { return __builtin_bit_cast(bf16x8, v); }
DI bf16x8 as_frag(u32x2 a, u32x2 b) { return __builtin_bit_cast(bf16x8, u32x4{a[0], a[1], b[0], b[1]}); }

#define PH 12
constexpr int NW = 12, SW = (NBK + NW - 1) / NW, PASSES = 3, CAP = 2880, RCAP = 3200, QFAST = PASSES * 64;
constexpr int STGS = 1088, STGW = 4 * STGS, MROW = 68;
constexpr u32 POFF = 244;
constexpr u32 L_WX = 0;
constexpr u32 L_EA = 256;
constexpr u32 L_OH = L_EA + RCAP * 8;
constexpr u32 L_ZERO = L_OH + 256;
constexpr u32 L_SINK = L_ZERO + 1024;
constexpr u32 L_CNT = L_ZERO + 3328;
constexpr u32 L_MSK = L_CNT + 1280;
constexpr u32 L_FLG = L_MSK + 256;
constexpr u32 L_NST = L_FLG + 64;
constexpr u32 L_PD = L_NST + 1280;
constexpr u32 L_G = 33024;
constexpr u32 L_HR = L_G + 56 * 128;
constexpr u32 L_STG = L_HR + 6144;
constexpr u32 L_S = L_STG + NW * STGW;
constexpr u32 L_DV = L_S + (CAP + 144) * 8;
constexpr u32 L_WH = L_DV + (CAP + 208) * 8;
constexpr u32 L_END = L_WH + 16384;
constexpr u32 PART_P = 8 * MROW * 4;
constexpr u32 L_SEA = L_EA, L_HIST = L_SEA + CHS * 8, L_OFFS = L_HIST + 1024;
constexpr u32 L_POS = L_STG, L_SIDX = L_POS + 59 * 1024, L_WE = L_SIDX + CHS * 4;
constexpr u32 L_D = L_WE + 17408, DSTR = QFAST * 4;
constexpr u32 SY0 = L_FLG + 16, SY1A = L_FLG + 20, SY1B = L_FLG + 24, SY2 = L_FLG + 28, SYW = L_FLG + 32;
static_assert(L_ZERO % 256 == 0 && L_STG % 256 == 0 && L_END <= 163840 && L_PD + 640 <= L_G && L_ZERO + 16 < 0x7F80, "lds layout");
static_assert(64 * NW * 2 <= CAP && CAP % 32 == 0 && L_OFFS + 1024 <= L_OH && L_D + NW * DSTR <= L_WH, "aliases");

DI void arrive(u32 addr, int lane) {
  asm volatile("s_waitcnt lgkmcnt(0)" ::: "memory");
  if (lane == 0) asm volatile("ds_add_u32 %0, %1" ::"v"(addr), "v"(1u) : "memory");
}
DI void await(u32 addr, u32 n) {
  while ((u32)__builtin_amdgcn_readfirstlane(__hip_atomic_load((LAS u32*)(size_t)addr, __ATOMIC_RELAXED, __HIP_MEMORY_SCOPE_WORKGROUP)) < n) __builtin_amdgcn_s_sleep(1);
  asm volatile("" ::: "memory");
}
DI void pos_piece(int j, int lane, const GAS void* pos) {
  j = j >= 59 ? j - 59 : j;
  dma16(pos, umin((u32)(j * 1024 + lane * 16), (u32)(NN * 6 - 16)), L_POS + (u32)j * 1024);
}
#define MFMA16(a, b, c) __builtin_amdgcn_mfma_f32_16x16x32_bf16(a, b, c, 0, 0, 0)
constexpr u32 SPIN_CAP = 1u << 22;

template <int P>
DI void flatten(u32 te, int seg, int lane, int c, u32 dsc, u32 vbase, u32 (&ro)[P], u32& Tw, u32& nv, u32& rbase) {
  const u32 scnt = ((te >> 12) + 1) >> 1;
  const u32 sincl = scan_add(scnt);
  const u32 sexcl = sincl - scnt;
  Tw = __builtin_amdgcn_readlane(sincl, 63);
  const u32 delta = ((u32)seg * CHS + (te & 0xfffu) - 2 * sexcl) * 8;
  if (scnt > 0 && sexcl < vbase + 64 * P) {
    const u32 pp = sexcl > vbase ? sexcl - vbase : 0u;
    __hip_atomic_fetch_max((LAS u32*)(size_t)(dsc + pp * 4), delta, __ATOMIC_RELAXED, __HIP_MEMORY_SCOPE_WAVEFRONT);
  }
  nv = Tw > vbase ? umin(Tw - vbase, 64 * P) : 0u;
  asm volatile("" ::: "memory");
  u32 carry = 0;
#pragma unroll
  for (int k = 0; k < P; ++k) ro[k] = lds_ld<u32>(dsc + (64 * k + lane) * 4);
  u32 rb = lds_add(lane == 0 ? L_FLG + 4 : L_FLG + 40, lane == 0 ? 2 * nv : 0u);
#pragma unroll
  for (int k = 0; k < P; ++k) {
    const u32 dl = umax(scan_max(ro[k]), carry);
    carry = __builtin_amdgcn_readlane(dl, 63);
    ro[k] = 64 * k + lane < nv ? 16 * (vbase + 64 * k + lane) + dl : ((u32)c * CHS + CH) * 8;
  }
  rbase = __builtin_amdgcn_readfirstlane(rb);
}
template <int P>
DI void fetch_index(const GAS char* ws, const u32 (&ro)[P], u32x2 (&ridx)[P]) {
#pragma unroll
  for (int k = 0; k < P; ++k) asm volatile("global_load_dwordx2 %0, %1, %2 sc1" : "=&v"(ridx[k]) : "v"(ro[k] >> 1), "s"(ws + WS_IDX) : "memory");
}
template <int P>
DI void fetch_attrs(int lane, const GAS char* ws, const u32 (&ro)[P], u32 nv, u32 rbase) {
#pragma unroll
  for (int k = 0; k < P; ++k) {
    if (64 * k + lane < nv) dma16_dev(ws + WS_EA, ro[k], L_EA + (rbase + 128 * k) * 8);
  }
}

template <int K>
DI void rank_records(int lane, int b, const u32x2* ridx, u32 (&rrank)[2 * K], u32 (&ca)[2 * K], u32 (&a6)[2 * K], u32x2 (&ps)[2 * K], u32x4 (&pd)[2 * K]) {
#pragma unroll
  for (int i = 0; i < 2 * K; ++i) {
    const u32 x = ridx[i >> 1][i & 1];
    const u32 n16 = (x >> 14) & 0x3f0u;
    ca[i] = (x ^ ((u32)b << 24)) < (40u << 18) ? n16 + (lane & 3) * 4 : 1024u + lane * 4;
    rrank[i] = lds_add(L_CNT + ca[i], 1u);
    a6[i] = (x & 0xffffu) * 6 + L_POS;
    const u32 pa = a6[i] & ~3u;
    ps[i] = u32x2{lds_ld<u32>(pa), lds_ld<u32>(pa + 4)};
    pd[i] = lds_ld<u32x4>(L_PD + n16);
  }
}
template <int K>
DI void scatter_records(u32 eptr, const u32x2* ridx, const u32 (&rrank)[2 * K], const u32 (&ca)[2 * K], const u32 (&a6)[2 * K], const u32x2 (&ps)[2 * K],
                        const u32x4 (&pd)[2 * K]) {
  u32 st[2 * K];
#pragma unroll
  for (int i = 0; i < 2 * K; ++i) st[i] = lds_ld<u32>(L_NST + ca[i]);
#pragma unroll
  for (int i = 0; i < 2 * K; ++i) {
    const u32 x = ridx[i >> 1][i & 1];
    const u32 sxy = __builtin_amdgcn_alignbyte(ps[i][1], ps[i][0], a6[i]), sz = __builtin_amdgcn_alignbyte(ps[i][1], ps[i][1], a6[i]);
    const float dx = __uint_as_float(pd[i][0]) - b2f(sxy), dy = __uint_as_float(pd[i][1]) - bhi(sxy), dz = __uint_as_float(pd[i][2]) - b2f(sz);
    const float d2 = (dx * dx + dy * dy + dz * dz) * NLOG2E;
    const u32 sa = st[i] + rrank[i] * 8;
    lds_st<u32x2>(sa, u32x2{(x & 0xffffu) | (pk2(d2, d2) & 0xffff0000u), eptr + (u32)((i >> 1) * 1024 + (i & 1) * 8) + pd[i][3]});
    lds_st<u32x2>(sa + ((ca[i] >> 1) & 0x1f8u) + (L_DV - L_S + 8), u32x2{pk2(1.0f, dx), pk2(dy, dz)});
  }
}
DI void scan_nodes(int lane, u32& nstart, u32& total) {
  const u32x4 cn = lds_ld<u32x4>(L_CNT + lane * 16);
  const u32 tot = cn[0] + cn[1] + cn[2] + cn[3];
  const u32 incl = scan_add(tot);
  nstart = incl - tot;
  total = __builtin_amdgcn_readlane(incl, 63);
  const u32 a = L_S + nstart * 8;
  lds_st<u32x4>(L_NST + lane * 16, u32x4{a, a + cn[0] * 8, a + (cn[0] + cn[1]) * 8, a + (tot - cn[3]) * 8});
  asm volatile("" ::: "memory");
}
DI void finish_lists(int lane, int w, u32 nstart, u32 total, u32& sbeg, u32& send) {
  if (w == NW - 1) {
    if (lane <= 41) lds_st<u32x2>(L_DV + (nstart + lane) * 8, u32x2{0, 0});
    lds_st<u32x2>(L_S + (total + lane) * 8, u32x2{0u, (L_ZERO - POFF) * 0x10001u});
  }
  const int ntiles = (int)(total + 31) >> 5;
  const int tq = ntiles / NW, tr = ntiles - tq * NW;
  const int tb = tq * w + (w < tr ? w : tr), tend = tb + tq + (w < tr ? 1 : 0);
  sbeg = (u32)tb * 32;
  send = umin((u32)tend * 32, total);
}

#define TILE_FIRST \
  "ds_read_u16 v92, %[s4a] offset:256\n\t" \
  "ds_read_u16 v94, %[s4a] offset:264\n\t" \
  "ds_read_u16 v96, %[s4a] offset:272\n\t" \
  "ds_read_u16 v98, %[s4a] offset:280\n\t" \
  "v_mov_b32 v93, %[lp]\n\t" \
  "v_mov_b32 v95, %[lp]\n\t" \
  "v_mov_b32 v97, %[lp]\n\t" \
  "v_mov_b32 v99, %[lp]\n\t" \
  "s_waitcnt lgkmcnt(0)\n\t" \
  "s_mov_b32 m0, %[stg]\n\t" \
  "s_nop 0\n\t" \
  "buffer_load_dwordx4 v[92:93], %[srd], 0 idxen offen sc1 lds\n\t" \
  "s_add_u32 m0, %[stg], 1088\n\t" \
  "s_nop 0\n\t" \
  "buffer_load_dwordx4 v[94:95], %[srd], 0 idxen offen sc1 lds\n\t" \
  "s_add_u32 m0, %[stg], 2176\n\t" \
  "s_nop 0\n\t" \
  "buffer_load_dwordx4 v[96:97], %[srd], 0 idxen offen sc1 lds\n\t" \
  "s_add_u32 m0, %[stg], 3264\n\t" \
  "s_nop 0\n\t" \
  "buffer_load_dwordx4 v[98:99], %[srd], 0 idxen offen sc1 lds\n\t"
#define TILE_FIRST_CLOBBERS "v92", "v93", "v94", "v95", "v96", "v97", "v98", "v99"
#define TILE_LOOP \
  "ds_read_u16 v100, %[s1a] offset:256\n\t" \
  "v_mov_b32 v93, %[lp]\n\t" \
  "v_mov_b32 v95, %[lp]\n\t" \
  "v_mov_b32 v97, %[lp]\n\t" \
  "v_mov_b32 v99, %[lp]\n\t" \
  "v_subrev_u32 v101, 64, %[lo]\n\t" \
  "v_subrev_u32 v102, 64, %[hi]\n\t" \
  "v_subrev_u32 v103, 128, %[lo]\n\t" \
  "v_subrev_u32 v104, 128, %[hi]\n\t" \
  "v_subrev_u32 v105, 192, %[lo]\n\t" \
  "v_subrev_u32 v106, 192, %[hi]\n\t" \
  "s_waitcnt lgkmcnt(0)\n\t" \
  "v_bfi_b32 v64, %[mh], v100, %[s1a]\n\t" \
  "ds_read_b64 v[88:89], v100 offset:244\n\t" \
  "ds_read_b64 v[90:91], v64 offset:252\n\t" \
  "ds_read_u16 v100, %[s1a] offset:512\n\t" \
  "ds_read_u16 v92, %[s4a] offset:512\n\t" \
  "ds_read_u16 v94, %[s4a] offset:520\n\t" \
  "ds_read_u16 v96, %[s4a] offset:528\n\t" \
  "ds_read_u16 v98, %[s4a] offset:536\n\t" \
  "v_add_u32 %[s1a], 0x100, %[s1a]\n\t" \
  "v_add_u32 %[s4a], 0x100, %[s4a]\n\t" \
  "1:\n\t" \
  "s_setprio 3\n\t" \
  "v_med3_i32 v64, %[da], %[lo], %[hi]\n\t" \
  "v_med3_i32 v65, %[da], v101, v102\n\t" \
  "v_med3_i32 v66, %[da], v103, v104\n\t" \
  "v_med3_i32 v67, %[da], v105, v106\n\t" \
  "ds_read_b64_tr_b16 v[80:81], v64 offset:0\n\t" \
  "ds_read_b64_tr_b16 v[82:83], v65 offset:64\n\t" \
  "ds_read_b64_tr_b16 v[84:85], v66 offset:128\n\t" \
  "ds_read_b64_tr_b16 v[86:87], v67 offset:192\n\t" \
  "v_add_u32 %[da], 0x100, %[da]\n\t" \
  "s_waitcnt vmcnt(0)\n\t" \
  "ds_read_b64_tr_b16 v[32:33], %[cb] offset:0\n\t" \
  "ds_read_b64_tr_b16 v[34:35], %[cb] offset:2176\n\t" \
  "ds_read_b64_tr_b16 v[36:37], %[cb] offset:256\n\t" \
  "ds_read_b64_tr_b16 v[38:39], %[cb] offset:2432\n\t" \
  "ds_read_b64_tr_b16 v[40:41], %[cb] offset:512\n\t" \
  "ds_read_b64_tr_b16 v[42:43], %[cb] offset:2688\n\t" \
  "ds_read_b64_tr_b16 v[44:45], %[cb] offset:768\n\t" \
  "ds_read_b64_tr_b16 v[46:47], %[cb] offset:2944\n\t" \
  "ds_read_b64_tr_b16 v[48:49], %[cb] offset:64\n\t" \
  "ds_read_b64_tr_b16 v[50:51], %[cb] offset:2240\n\t" \
  "ds_read_b64_tr_b16 v[52:53], %[cb] offset:320\n\t" \
  "ds_read_b64_tr_b16 v[54:55], %[cb] offset:2496\n\t" \
  "ds_read_b64_tr_b16 v[56:57], %[cb] offset:576\n\t" \
  "ds_read_b64_tr_b16 v[58:59], %[cb] offset:2752\n\t" \
  "ds_read_b64_tr_b16 v[60:61], %[cb] offset:832\n\t" \
  "ds_read_b64_tr_b16 v[62:63], %[cb] offset:3008\n\t" \
  "s_waitcnt lgkmcnt(8)\n\t" \
  "v_mfma_f32_32x32x16_bf16 v[32:47], v[88:91], %[wf0], v[32:47]\n\t" \
  "s_sub_u32 %[rem], %[rem], 1\n\t" \
  "s_cmp_eq_u32 %[rem], 0\n\t" \
  "s_waitcnt lgkmcnt(0)\n\t" \
  "v_mfma_f32_32x32x16_bf16 v[48:63], v[88:91], %[wf1], v[48:63]\n\t" \
  "s_cbranch_scc1 2f\n\t" \
  "s_mov_b32 m0, %[stg]\n\t" \
  "s_nop 0\n\t" \
  "buffer_load_dwordx4 v[92:93], %[srd], 0 idxen offen sc1 lds\n\t" \
  "s_add_u32 m0, %[stg], 1088\n\t" \
  "s_nop 0\n\t" \
  "buffer_load_dwordx4 v[94:95], %[srd], 0 idxen offen sc1 lds\n\t" \
  "s_add_u32 m0, %[stg], 2176\n\t" \
  "s_nop 0\n\t" \
  "buffer_load_dwordx4 v[96:97], %[srd], 0 idxen offen sc1 lds\n\t" \
  "s_add_u32 m0, %[stg], 3264\n\t" \
  "s_nop 0\n\t" \
  "buffer_load_dwordx4 v[98:99], %[srd], 0 idxen offen sc1 lds\n\t" \
  "v_bfi_b32 v64, %[mh], v100, %[s1a]\n\t" \
  "ds_read_b64 v[88:89], v100 offset:244\n\t" \
  "ds_read_b64 v[90:91], v64 offset:252\n\t" \
  "ds_read_u16 v100, %[s1a] offset:512\n\t" \
  "ds_read_u16 v92, %[s4a] offset:512\n\t" \
  "ds_read_u16 v94, %[s4a] offset:520\n\t" \
  "ds_read_u16 v96, %[s4a] offset:528\n\t" \
  "ds_read_u16 v98, %[s4a] offset:536\n\t" \
  "v_add_u32 %[s1a], 0x100, %[s1a]\n\t" \
  "v_add_u32 %[s4a], 0x100, %[s4a]\n\t" \
  "s_setprio 2\n\t" \
  "s_cmp_lg_u32 %[rem], 1\n\t" \
  "s_cbranch_scc1 3f\n\t" \
  "s_setprio 1\n\t" \
  "s_branch 3f\n\t" \
  "2:\n\t" \
  "s_setprio 0\n\t" \
  "s_nop 7\n\t" \
  "3:\n\t" \
  "v_exp_f32 v64, v32\n\t" \
  "v_exp_f32 v65, v33\n\t" \
  "v_exp_f32 v66, v34\n\t" \
  "v_exp_f32 v67, v35\n\t" \
  "v_exp_f32 v68, v36\n\t" \
  "v_exp_f32 v69, v37\n\t" \
  "v_exp_f32 v70, v38\n\t" \
  "v_exp_f32 v71, v39\n\t" \
  "v_exp_f32 v72, v40\n\t" \
  "v_exp_f32 v73, v41\n\t" \
  "v_exp_f32 v74, v42\n\t" \
  "v_exp_f32 v75, v43\n\t" \
  "v_exp_f32 v76, v44\n\t" \
  "v_exp_f32 v77, v45\n\t" \
  "v_exp_f32 v78, v46\n\t" \
  "v_exp_f32 v79, v47\n\t" \
  "v_pk_add_f32 v[64:65], v[64:65], 1.0 op_sel_hi:[1,0]\n\t" \
  "v_pk_add_f32 v[66:67], v[66:67], 1.0 op_sel_hi:[1,0]\n\t" \
  "v_pk_add_f32 v[68:69], v[68:69], 1.0 op_sel_hi:[1,0]\n\t" \
  "v_pk_add_f32 v[70:71], v[70:71], 1.0 op_sel_hi:[1,0]\n\t" \
  "v_pk_add_f32 v[72:73], v[72:73], 1.0 op_sel_hi:[1,0]\n\t" \
  "v_pk_add_f32 v[74:75], v[74:75], 1.0 op_sel_hi:[1,0]\n\t" \
  "v_pk_add_f32 v[76:77], v[76:77], 1.0 op_sel_hi:[1,0]\n\t" \
  "v_pk_add_f32 v[78:79], v[78:79], 1.0 op_sel_hi:[1,0]\n\t" \
  "v_rcp_f32 v64, v64\n\t" \
  "v_rcp_f32 v65, v65\n\t" \
  "v_rcp_f32 v66, v66\n\t" \
  "v_rcp_f32 v67, v67\n\t" \
  "v_rcp_f32 v68, v68\n\t" \
  "v_rcp_f32 v69, v69\n\t" \
  "v_rcp_f32 v70, v70\n\t" \
  "v_rcp_f32 v71, v71\n\t" \
  "v_rcp_f32 v72, v72\n\t" \
  "v_rcp_f32 v73, v73\n\t" \
  "v_rcp_f32 v74, v74\n\t" \
  "v_rcp_f32 v75, v75\n\t" \
  "v_rcp_f32 v76, v76\n\t" \
  "v_rcp_f32 v77, v77\n\t" \
  "v_rcp_f32 v78, v78\n\t" \
  "v_rcp_f32 v79, v79\n\t" \
  "v_pk_mul_f32 v[32:33], v[32:33], v[64:65]\n\t" \
  "v_pk_mul_f32 v[34:35], v[34:35], v[66:67]\n\t" \
  "v_pk_mul_f32 v[36:37], v[36:37], v[68:69]\n\t" \
  "v_pk_mul_f32 v[38:39], v[38:39], v[70:71]\n\t" \
  "v_pk_mul_f32 v[40:41], v[40:41], v[72:73]\n\t" \
  "v_pk_mul_f32 v[42:43], v[42:43], v[74:75]\n\t" \
  "v_pk_mul_f32 v[44:45], v[44:45], v[76:77]\n\t" \
  "v_pk_mul_f32 v[46:47], v[46:47], v[78:79]\n\t" \
  "v_cvt_pk_bf16_f32 v32, v32, v33\n\t" \
  "v_cvt_pk_bf16_f32 v33, v34, v35\n\t" \
  "v_cvt_pk_bf16_f32 v34, v36, v37\n\t" \
  "v_cvt_pk_bf16_f32 v35, v38, v39\n\t" \
  "v_cvt_pk_bf16_f32 v36, v40, v41\n\t" \
  "v_cvt_pk_bf16_f32 v37, v42, v43\n\t" \
  "v_cvt_pk_bf16_f32 v38, v44, v45\n\t" \
  "v_cvt_pk_bf16_f32 v39, v46, v47\n\t" \
  "v_exp_f32 v64, v48\n\t" \
  "v_exp_f32 v65, v49\n\t" \
  "v_mfma_f32_32x32x16_bf16 v[0:15], v[80:83], v[32:35], v[0:15]\n\t" \
  "v_exp_f32 v66, v50\n\t" \
  "v_exp_f32 v67, v51\n\t" \
  "v_exp_f32 v68, v52\n\t" \
  "v_exp_f32 v69, v53\n\t" \
  "v_mfma_f32_32x32x16_bf16 v[0:15], v[84:87], v[36:39], v[0:15]\n\t" \
  "v_exp_f32 v70, v54\n\t" \
  "v_exp_f32 v71, v55\n\t" \
  "v_exp_f32 v72, v56\n\t" \
  "v_exp_f32 v73, v57\n\t" \
  "v_exp_f32 v74, v58\n\t" \
  "v_exp_f32 v75, v59\n\t" \
  "v_exp_f32 v76, v60\n\t" \
  "v_exp_f32 v77, v61\n\t" \
  "v_exp_f32 v78, v62\n\t" \
  "v_exp_f32 v79, v63\n\t" \
  "v_pk_add_f32 v[64:65], v[64:65], 1.0 op_sel_hi:[1,0]\n\t" \
  "v_pk_add_f32 v[66:67], v[66:67], 1.0 op_sel_hi:[1,0]\n\t" \
  "v_pk_add_f32 v[68:69], v[68:69], 1.0 op_sel_hi:[1,0]\n\t" \
  "v_pk_add_f32 v[70:71], v[70:71], 1.0 op_sel_hi:[1,0]\n\t" \
  "v_pk_add_f32 v[72:73], v[72:73], 1.0 op_sel_hi:[1,0]\n\t" \
  "v_pk_add_f32 v[74:75], v[74:75], 1.0 op_sel_hi:[1,0]\n\t" \
  "v_pk_add_f32 v[76:77], v[76:77], 1.0 op_sel_hi:[1,0]\n\t" \
  "v_pk_add_f32 v[78:79], v[78:79], 1.0 op_sel_hi:[1,0]\n\t" \
  "v_rcp_f32 v64, v64\n\t" \
  "v_rcp_f32 v65, v65\n\t" \
  "v_rcp_f32 v66, v66\n\t" \
  "v_rcp_f32 v67, v67\n\t" \
  "v_rcp_f32 v68, v68\n\t" \
  "v_rcp_f32 v69, v69\n\t" \
  "v_rcp_f32 v70, v70\n\t" \
  "v_rcp_f32 v71, v71\n\t" \
  "v_rcp_f32 v72, v72\n\t" \
  "v_rcp_f32 v73, v73\n\t" \
  "v_rcp_f32 v74, v74\n\t" \
  "v_rcp_f32 v75, v75\n\t" \
  "v_rcp_f32 v76, v76\n\t" \
  "v_rcp_f32 v77, v77\n\t" \
  "v_rcp_f32 v78, v78\n\t" \
  "v_rcp_f32 v79, v79\n\t" \
  "v_pk_mul_f32 v[48:49], v[48:49], v[64:65]\n\t" \
  "v_pk_mul_f32 v[50:51], v[50:51], v[66:67]\n\t" \
  "v_pk_mul_f32 v[52:53], v[52:53], v[68:69]\n\t" \
  "v_pk_mul_f32 v[54:55], v[54:55], v[70:71]\n\t" \
  "v_pk_mul_f32 v[56:57], v[56:57], v[72:73]\n\t" \
  "v_pk_mul_f32 v[58:59], v[58:59], v[74:75]\n\t" \
  "v_pk_mul_f32 v[60:61], v[60:61], v[76:77]\n\t" \
  "v_pk_mul_f32 v[62:63], v[62:63], v[78:79]\n\t" \
  "v_cvt_pk_bf16_f32 v48, v48, v49\n\t" \
  "v_cvt_pk_bf16_f32 v49, v50, v51\n\t" \
  "v_cvt_pk_bf16_f32 v50, v52, v53\n\t" \
  "v_cvt_pk_bf16_f32 v51, v54, v55\n\t" \
  "v_cvt_pk_bf16_f32 v52, v56, v57\n\t" \
  "v_cvt_pk_bf16_f32 v53, v58, v59\n\t" \
  "v_cvt_pk_bf16_f32 v54, v60, v61\n\t" \
  "v_cvt_pk_bf16_f32 v55, v62, v63\n\t" \
  "v_mfma_f32_32x32x16_bf16 v[16:31], v[80:83], v[48:51], v[16:31]\n\t" \
  "s_cmp_lg_u32 %[rem], 0\n\t" \
  "v_mfma_f32_32x32x16_bf16 v[16:31], v[84:87], v[52:55], v[16:31]\n\t" \
  "s_cbranch_scc1 1b\n\t" \
  "s_nop 11\n\t"
#define TILE_LOOP_CLOBBERS "v32", "v33", "v34", "v35", "v36", "v37", "v38", "v39", "v40", "v41", "v42", "v43", "v44", "v45", "v46", "v47", "v48", "v49", "v50", "v51", "v52", "v53", "v54", "v55", "v56", "v57", "v58", "v59", "v60", "v61", "v62", "v63", "v64", "v65", "v66", "v67", "v68", "v69", "v70", "v71", "v72", "v73", "v74", "v75", "v76", "v77", "v78", "v79", "v80", "v81", "v82", "v83", "v84", "v85", "v86", "v87", "v88", "v89", "v90", "v91", "v92", "v93", "v94", "v95", "v96", "v97", "v98", "v99", "v100", "v101", "v102", "v103", "v104", "v105", "v106"

DI u32x4 make_srd(const GAS char* ws) {
  const u64 bp = (u64)(ws + WS_BP);
  return u32x4{(u32)bp, (u32)(bp >> 32) | (128u << 16), (u32)NN, 0x00020000u};
}
struct Win { u32 cbase, s4a, s1a, mh, lp; int lo, hi, da; bf16x8 wf0, wf1; f32x16 acc0, acc1; };
DI void init_window(Win& W, int lane, u32 stg) {
  const int hh = lane >> 5, blk = (lane >> 4) & 1, q = (lane >> 2) & 3, p = lane & 3;
  W.cbase = (q & 1) ? stg + 128 * hh + 32 * blk + 8 * p + (q >> 1) * STGS : L_ZERO + 128 * (1 - hh);
  W.s4a = L_S - 256 + (u32)(4 * (lane >> 3)) * 8; W.s1a = L_S - 256 + (u32)(lane & 31) * 8 + 4 + 2 * hh;
  W.da = (int)L_DV + 8 * (4 * hh + q + 1);
  W.mh = hh ? ~0u : 0u; W.lp = (lane & 7) * 16;
#pragma unroll
  for (int i = 0; i < 16; ++i) { W.acc0[i] = 0.f; W.acc1[i] = 0.f; }
  asm volatile("" : "+{v[0:15]}"(W.acc0), "+{v[16:31]}"(W.acc1), "+v"(W.cbase), "+v"(W.s4a), "+v"(W.s1a), "+v"(W.mh), "+v"(W.lp), "+v"(W.da));
}
DI void setup_window(Win& W, int lane, int nn, int base, u32 sbeg) {
  const int blk = (lane >> 4) & 1, q = (lane >> 2) & 3, p = lane & 3;
  const u32 s8 = (sbeg & ~31u) * 8;
  W.s4a += s8; W.s1a += s8;
  const int rb = lane < 32 ? 48 : base;
  const int r0 = rb + ((q - rb) & 7), r1 = rb + ((4 + q - rb) & 7);
  const u32 a0 = L_G + r0 * 128 + 32 * blk + 8 * p, a1 = L_G + r1 * 128 + 32 * blk + 8 * p;
  W.wf0 = as_frag(tr_read(a0), tr_read(a1)); W.wf1 = as_frag(tr_read(a0 + 64), tr_read(a1 + 64));
  int np = base + ((4 * blk + p - base) & 7); np = np < nn ? np : nn;
  W.lo = (int)(L_DV - L_S) + (int)lds_ld<u32>(L_NST + np * 16) + 8 * np;
  W.hi = (int)(L_DV - L_S + 8) + (int)lds_ld<u32>(L_NST + np * 16 + 16) + 8 * np;
  W.da += (int)s8 + 8 * np;
  asm volatile("" : "+v"(W.s4a), "+v"(W.s1a), "+v"(W.lo), "+v"(W.hi), "+v"(W.da), "+v"(W.wf0), "+v"(W.wf1));
}
DI void first_tile(const Win& W, u32 stg, u32x4 srd) {
  asm volatile(TILE_FIRST ::[s4a] "v"(W.s4a), [lp] "v"(W.lp), [srd] "s"(srd), [stg] "s"(stg) : "memory", "scc", TILE_FIRST_CLOBBERS);
}
DI void run_tiles(Win& W, u32 stg, u32x4 srd, u32 sbeg, u32 wend) {
  u32 rem = ((wend + 31) >> 5) - (sbeg >> 5);
  asm volatile(TILE_LOOP
               : "+{v[0:15]}"(W.acc0), "+{v[16:31]}"(W.acc1), [rem] "+s"(rem), [s4a] "+v"(W.s4a), [s1a] "+v"(W.s1a), [da] "+v"(W.da)
               : [srd] "s"(srd), [stg] "s"(stg), [cb] "v"(W.cbase), [lp] "v"(W.lp), [mh] "v"(W.mh), [lo] "v"(W.lo), [hi] "v"(W.hi), [wf0] "v"(W.wf0), [wf1] "v"(W.wf1)
               : "memory", "scc", TILE_LOOP_CLOBBERS);
}
DI void node_operands(int lane, int etile, int eT, f32x4& z, bf16x8 (&wh2)[2], u32x2& hr) {
  const int enl = etile * 16 + (lane & 15);
  const u32x4 hf0 = lds_ld<u32x4>(L_HR + enl * 128 + (lane >> 4) * 16), hf1 = lds_ld<u32x4>(L_HR + enl * 128 + (lane >> 4) * 16 + 64);
  hr = lds_ld<u32x2>(L_HR + enl * 128 + eT * 32 + (lane >> 4) * 8);
  const u32 o0 = L_WH + (u32)((8 * (lane >> 4) + ((lane >> 2) & 3)) * 128 + (16 * eT + 4 * (lane & 3)) * 2);
  z = f32x4{0.f, 0.f, 0.f, 0.f};
  z = MFMA16(as_frag(tr_read(o0), tr_read(o0 + 512)), as_frag(hf0), z);
  z = MFMA16(as_frag(tr_read(o0 + 4096), tr_read(o0 + 4608)), as_frag(hf1), z);
  wh2[0] = as_frag(tr_read(o0 + 8192), tr_read(o0 + 8704));
  wh2[1] = as_frag(tr_read(o0 + 12288), tr_read(o0 + 12800));
}

__global__ __launch_bounds__(NW * 64) void egnn_fused(const u16* h_, const u16* pos_, const void* ea_, const void* src_, const void* dst_,
                                                      const u16* we_, const u16* wx_, const u16* wh_, char* ws_, u16* out_) {
  STAMP_DECL
  STAMP(0);
  u64x8 ka; u64x2 kb;
  asm volatile("s_load_dwordx16 %0, %2, 0x0\n\ts_load_dwordx4 %1, %2, 0x40" : "=&s"(ka), "=&s"(kb) : "s"(__builtin_amdgcn_kernarg_segment_ptr()) : "memory");
  const int tid = threadIdx.x;
  int lane = tid & 63;
  const int w = __builtin_amdgcn_readfirstlane(tid >> 6);
  const int c = blockIdx.x;
  const int b = (c & 7) * 32 + (c >> 3);
  const int n0 = first_node(b), nn = first_node(b + 1) - n0;
  const u32 stg = L_STG + (u32)w * STGW, dsc = L_D + (u32)w * DSTR;
  {
    constexpr u32 NZ = (L_NST - L_ZERO) / 16;
    if (tid < NZ + 64) lds_st<u32x4>(tid < NZ ? L_ZERO + tid * 16 : L_HIST + (tid - NZ) * 16, u32x4{0, 0, 0, 0});
    else if (w == 10) lds_st<u32>(L_NST + 1024 + lane * 4, L_S + (CAP + 64) * 8);
    if (lane < 48) lds_st<u32x4>(dsc + lane * 16, u32x4{0, 0, 0, 0});
    if (w == 11) lds_st<u32>(L_OH + lane * 4, (((lane >> 3) & 3) == (lane & 3)) ? (((lane >> 2) & 1) ? 0x3F800000u : 0x3F80u) : 0u);
  }
  asm volatile("s_waitcnt lgkmcnt(0)" ::: "memory");
  __builtin_amdgcn_sched_barrier(0);
  asm volatile("" : "+s"(ka), "+s"(kb));
  const GAS u16* h = (const GAS u16*)ka[0]; const GAS u16* pos = (const GAS u16*)ka[1];
  const GAS char* ea4 = (const GAS char*)ka[2]; const GAS char* src4 = (const GAS char*)ka[3]; const GAS char* dst4 = (const GAS char*)ka[4];
  const GAS u16* we = (const GAS u16*)ka[5]; const GAS u16* wx = (const GAS u16*)ka[6]; const GAS u16* wh = (const GAS u16*)ka[7];
  GAS char* ws = (GAS char*)kb[0]; GAS u16* out = (GAS u16*)kb[1];
  const u32 epoch = *(const __attribute__((address_space(4))) u32*)(kb[0] + WS_EP);
  const u32 tag = epoch + 0x9E3779B1u, tag8 = (tag * 0x01010101u) & 0xff000000u;
  const int rot = ((c >> 3) * 15) >> 3;

  if (__builtin_expect(w < 10, 1)) {
    const bool act = tid < CH / 4;
    const u32 q = (u32)(c * (CH / 4) + (act ? tid : 0));
    u32x4 d, s, e0, e1;
    asm volatile("global_load_dwordx4 %0, %2, %3 nt\n\tglobal_load_dwordx4 %1, %2, %4 nt" : "=&v"(d), "=&v"(s) : "v"(q * 16), "s"(dst4), "s"(src4) : "memory");
    asm volatile("s_barrier" ::: "memory");
    asm volatile("global_load_dwordx4 %0, %2, %3 nt\n\tglobal_load_dwordx4 %1, %2, %3 offset:16 nt" : "=&v"(e0), "=&v"(e1) : "v"(q * 32), "s"(ea4) : "memory");
    STAMP(1);
    u32 bk[4], rk[4];
    WAITV(3); asm volatile("" : "+v"(d));
#pragma unroll
    for (int k = 0; k < 4; ++k) bk[k] = (d[k] * 16u) / 625u;
#pragma unroll
    for (int k = 0; k < 4; ++k) rk[k] = lds_add(L_HIST + bk[k] * 4, act ? 1u : 0u);
    arrive(SY0, lane);
#pragma unroll
    for (int k = 0; k < 4; ++k) d[k] = ((d[k] - (u32)first_node(bk[k])) << 18) | (bk[k] << 24) | (act ? 0u : 0xfc0000u);
    STAMP(2);
    await(SY0, 10);
    const u32x4 cn = lds_ld<u32x4>(L_HIST + lane * 16);
    const u32 tot = cn[0] + cn[1] + cn[2] + cn[3];
    const u32 ex = scan_add(tot) - tot;
    lds_st<u32x4>(L_OFFS + lane * 16, u32x4{ex, ex + cn[0], ex + cn[0] + cn[1], ex + cn[0] + cn[1] + cn[2]});
    asm volatile("" ::: "memory");
#pragma unroll
    for (int k = 0; k < 4; ++k) rk[k] = umin(rk[k] + lds_ld<u32>(L_OFFS + bk[k] * 4) + (act ? 0u : 65536u), CH + (k & 1)) * 8;
    WAITV(2); asm volatile("" : "+v"(s));
#pragma unroll
    for (int k = 0; k < 4; ++k) lds_st<u32>(L_SIDX + (rk[k] >> 1), s[k] | d[k]);
    WAITV(1); asm volatile("" : "+v"(e0));
    lds_st<u32x2>(L_SEA + rk[0], u32x2{e0[0], e0[1]}); lds_st<u32x2>(L_SEA + rk[1], u32x2{e0[2], e0[3]});
    WAITV(0); asm volatile("" : "+v"(e1));
    lds_st<u32x2>(L_SEA + rk[2], u32x2{e1[0], e1[1]}); lds_st<u32x2>(L_SEA + rk[3], u32x2{e1[2], e1[3]});
    arrive(SY1A, lane);
    STAMP(3);
    await(SY1A, 10);
    st16_dev(ws, WS_IDX + (u32)c * CHS * 4 + tid * 16, lds_ld<u32x4>(L_SIDX + tid * 16));
    u32x4 x = lds_ld<u32x4>(L_SEA + tid * 16), y = lds_ld<u32x4>(L_SEA + 10240 + tid * 16);
    u32x4 t0 = {(ex | (cn[0] << 12)) | tag8, tag, ((ex + cn[0]) | (cn[1] << 12)) | tag8, tag};
    u32x4 t1 = {((ex + cn[0] + cn[1]) | (cn[2] << 12)) | tag8, tag, ((ex + tot - cn[3]) | (cn[3] << 12)) | tag8, tag};
    asm volatile("s_waitcnt vmcnt(0)" : "+v"(x), "+v"(y), "+v"(t0), "+v"(t1)::"memory");
    if (__builtin_amdgcn_readfirstlane(lds_add(lane == 0 ? SY1B : L_FLG + 40, lane == 0 ? 1u : 0u)) == 9) {
      st16_dev(ws, WS_TAB + (u32)c * 2048 + lane * 16, t0);
      st16_dev(ws, WS_TAB + (u32)c * 2048 + 1024 + lane * 16, t1);
      arrive(SY1B, lane);
    }
    await(SY1B, 11);
    st16_dev(ws, WS_EA + (u32)c * CHS * 8 + tid * 16, x);
    st16_dev(ws, WS_EA + (u32)c * CHS * 8 + 10240 + tid * 16, y);
    STAMP(4);
  } else {
    const int m = 11 - w;
    u32x4 hf[3][2];
#pragma unroll
    for (int t = 0; t < 3; ++t) {
      const int l = 16 * t + (lane & 15);
      const GAS u16* hp = h + (n0 + (l < nn ? l : 0)) * 64 + 8 * (lane >> 4);
      hf[t][0] = *(const GAS u32x4*)hp; hf[t][1] = *(const GAS u32x4*)(hp + 32);
    }
    const GAS u16* pp = m ? pos + (n0 + (lane < nn ? lane : 0)) * 3 : we + 128 * 64 + lane;
    u32 pv[5];
#pragma unroll
    for (int i = 0; i < 5; ++i) pv[i] = pp[m ? (i < 3 ? i : 0) : 64 * i];
#pragma unroll
    for (int i = 0; i < 8; ++i) dma16(we, (u32)((8 * m + i) * 1024 + lane * 16), L_WE + (u32)(8 * m + i) * 1024);
    asm volatile("s_mov_b32 m0, %2\n\ts_nop 0\n\tglobal_load_lds_dword %0, %1" ::"v"(m ? umin((u32)((c >> 3) * 1920 + (lane & 31) * 64), (u32)(NN * 6 - 4)) : (u32)((c >> 3) * 512 + (lane & 7) * 64)),
                 "s"(m ? pos : wh), "s"(L_SINK) : "memory");
    asm volatile("s_barrier" ::: "memory");
    STAMP(1);
    asm volatile("s_waitcnt vmcnt(0)" : "+v"(hf[0][0]), "+v"(hf[0][1]), "+v"(hf[1][0]), "+v"(hf[1][1]), "+v"(hf[2][0]), "+v"(hf[2][1]), "+v"(pv[0]), "+v"(pv[1]),
                 "+v"(pv[2]), "+v"(pv[3]), "+v"(pv[4])::"memory");
#pragma unroll 1
    for (int i = 0; i < PH; ++i) pos_piece(rot + m * PH + i, lane, pos);
    STAMP(2);
    const u32 a0 = L_WE + 8192 * m + (u32)((8 * (lane >> 4) + ((lane >> 2) & 3)) * 128 + 16 * (lane & 3));
#pragma unroll
    for (int hv = 0; hv < 2; ++hv) {
      bf16x8 wfr[2][2];
#pragma unroll
      for (int T = 0; T < 2; ++T)
#pragma unroll
        for (int ks = 0; ks < 2; ++ks) { const u32 a = a0 + 4096 * ks + 64 * hv + 8 * T; wfr[T][ks] = as_frag(tr_read(a), tr_read(a + 512)); }
#pragma unroll
      for (int t = 0; t < 3; ++t) {
        u32x4 v;
#pragma unroll
        for (int T = 0; T < 2; ++T) {
          f32x4 acc = {0.f, 0.f, 0.f, 0.f};
          acc = MFMA16(wfr[T][0], as_frag(hf[t][0]), acc); acc = MFMA16(wfr[T][1], as_frag(hf[t][1]), acc);
          v[2 * T] = pk2(acc[0] * NLOG2E, acc[1] * NLOG2E); v[2 * T + 1] = pk2(acc[2] * NLOG2E, acc[3] * NLOG2E);
        }
        const int l = 16 * t + (lane & 15);
        if (m) { if (l < nn) st16_dev(ws, WS_BP + (u32)(n0 + l) * 128 + (lane >> 4) * 16 + 64 * hv, v); }
        else lds_st<u32x4>(L_G + l * 128 + (lane >> 4) * 16 + 64 * hv, v);
      }
    }
    STAMP(3);
    if (m) {
      if (lane < 40) lds_st<u32x4>(L_PD + lane * 16, u32x4{pv[0] << 16, pv[1] << 16, pv[2] << 16, (L_OH - POFF + (lane & 7) * 16) << 16});
    } else {
#pragma unroll
      for (int r = 0; r < 8; ++r) lds_st<u16>(L_G + (48 + r) * 128 + lane * 2, r < 4 ? (u16)pk2(b2f(pv[1 + r]) * NLOG2E, 0.f) : (r == 5 ? (u16)pv[0] : (u16)0));
#pragma unroll
      for (int t = 0; t < 3; ++t) {
        lds_st<u32x4>(L_HR + (16 * t + (lane & 15)) * 128 + (lane >> 4) * 16, hf[t][0]); lds_st<u32x4>(L_HR + (16 * t + (lane & 15)) * 128 + (lane >> 4) * 16 + 64, hf[t][1]);
      }
    }
    STAMP(4);
  }
  asm volatile("s_waitcnt vmcnt(0)" ::: "memory");
  if (__builtin_amdgcn_readfirstlane(lds_add(lane == 0 ? SY2 : L_FLG + 40, lane == 0 ? 1u : 0u)) == NW - 1) {
    st16_dev(ws, WS_FLE + (u32)c * 1024 + lane * 16, u32x4{~tag, tag, ~tag, tag});
    arrive(SY2, lane);
  }
  Win W;
  init_window(W, lane, stg);
  await(SY2, NW + 1);
  STAMP(5);

  const int seg = w * SW + lane;
  const bool sv = lane < SW && seg < NBK;
  const int bs = 2 * (b >> 2) + (b & 1) + 128 * ((b >> 1) & 1);
  u32 te = 0;
  {
    u64 g;
    asm volatile("global_load_dwordx2 %0, %1, %2 sc1" : "=&v"(g) : "v"((u32)(((sv ? seg : c) * NBK + bs) * 8)), "s"(ws + WS_TAB) : "memory");
    static_assert(2 * PH + 3 * NW >= 59, "position pieces");
#pragma unroll
    for (int i = 0; i < 3; ++i) pos_piece(rot + umin(2 * PH + w * 3 + i, 58), lane, pos);
    WAITV(3); asm volatile("" : "+v"(g));
#pragma unroll 1
    for (u32 spins = 0; spins < SPIN_CAP; ++spins) {
      const u32 x = (u32)g ^ tag8;
      const bool ok = (u32)(g >> 32) == tag && x < 0x1000000u;
      te = ok && sv ? x : 0u;
      if (__all(ok || !sv)) break;
      __builtin_amdgcn_s_sleep(2);
      if (!ok && sv) g = __hip_atomic_load((GAS u64*)(ws + WS_TAB) + seg * NBK + bs, RLX_AGENT);
    }
    __builtin_amdgcn_fence(__ATOMIC_ACQUIRE, "wavefront");
  }
  u32x2 ridx[PASSES];
  u32 ro[PASSES], Tw, nv, rbase;
  flatten<PASSES>(te, seg, lane, c, dsc, 0u, ro, Tw, nv, rbase);
  fetch_index<PASSES>(ws, ro, ridx);
  u64 fle;
  asm volatile("global_load_dwordx2 %0, %1, %2 sc1" : "=&v"(fle) : "v"((u32)((sv ? seg : c) * 1024 + (c & 7) * 128)), "s"(ws + WS_FLE) : "memory");
  const bool rok = rbase + 2 * nv <= (u32)RCAP;
  if ((Tw > (u32)QFAST || !rok) && lane == 0) lds_st<u32>(L_FLG, 1u);
  WAITV(4);
  STAMP(6);
  wg_barrier();
  const int etile = w % 3, eT = w / 3;
  u32 pold = lds_ld<u16>(L_POS + (u32)umin(n0 * 3 + w * 16 + (lane >> 2), NN * 3 - 1) * 2);
  WAITV(1);
#pragma unroll
  for (int k = 0; k < PASSES; ++k) asm volatile("" : "+v"(ridx[k]));
  STAMP(7);

  bool rare;
  u32 ma[2], pa[2], mk = 0, pk = 0;
  ma[0] = ma[1] = pa[0] = pa[1] = L_ZERO;
  {
    const u32 eptr = L_EA - POFF + (rbase + 2 * lane) * 8;
    u32 rrank[4], ca[4], a6[4], rrank2[2], ca2[2], a62[2];
    u32x2 ps[4], ps2[2];
    u32x4 pd[4], pd2[2];
    rank_records<2>(lane, b, ridx, rrank, ca, a6, ps, pd);
    if (nv > 128) rank_records<1>(lane, b, ridx + 2, rrank2, ca2, a62, ps2, pd2);
    WAITV(0); asm volatile("" : "+v"(fle));
    bool eok = !sv || fle == (((u64)tag << 32) | ~tag);
    const bool early = __all(eok);
    if (early && rok) fetch_attrs<PASSES>(lane, ws, ro, nv, rbase);
    STAMP(8);
    wg_barrier();
    STAMP(9);
    {
      asm volatile("s_mov_b32 m0, %2\n\ts_nop 0\n\tglobal_load_lds_dword %0, %1 sc1" ::"v"(umin((u32)((c >> 3) * 40000 + tid * 64), (u32)(NN * 128 - 64))), "s"(ws + WS_BP), "s"(L_SINK) : "memory");
      const u32 flg = lds_ld<u32>(L_FLG);
      u32 nstart, total;
      scan_nodes(lane, nstart, total);
      if (c == 0 && tid == 64) __hip_atomic_store((GAS u32*)(ws + WS_EP), tag, RLX_AGENT);
      rare = __builtin_amdgcn_readfirstlane(flg) != 0 || total > (u32)CAP;
      u32 sbeg = 0, send = 0;
      int fn = 0, ln = 0;
      if (!rare) {
        scatter_records<2>(eptr, ridx, rrank, ca, a6, ps, pd);
        if (nv > 128) scatter_records<1>(eptr + 2048, ridx + 2, rrank2, ca2, a62, ps2, pd2);
        finish_lists(lane, w, nstart, total, sbeg, send);
        fn = __builtin_popcountll(__ballot(nstart <= sbeg && lane < nn)) - 1;
        ln = __builtin_popcountll(__ballot(nstart < send && lane < nn)) - 1;
        if (sbeg < send) {
          if (ln - fn >= 8) { if (lane == 0) lds_st<u32>(L_FLG + 8, 1u); }
          else if (lane >= fn && lane <= ln) __hip_atomic_fetch_or((LAS u32*)(size_t)(L_MSK + lane * 4), 1u << w, __ATOMIC_RELAXED, __HIP_MEMORY_SCOPE_WORKGROUP);
        }
      }
      STAMP(10);
      if (!early) {
#pragma unroll 1
        for (u32 spins = 0; spins < SPIN_CAP; ++spins) {
          if (!eok) eok = __hip_atomic_load((GAS u64*)(ws + WS_FLE + seg * 1024 + (c & 7) * 128), RLX_AGENT) == (((u64)tag << 32) | ~tag);
          if (__all(eok)) break;
          __builtin_amdgcn_s_sleep(2);
        }
        __builtin_amdgcn_fence(__ATOMIC_ACQUIRE, "wavefront");
        if (!rare) fetch_attrs<PASSES>(lane, ws, ro, nv, rbase);
      }
#pragma unroll
      for (int i = 0; i < 2; ++i) {
        const int j = w + NW * i;
        if (j < 16) dma16(wh, (u32)(j * 1024 + lane * 16), L_WH + (u32)j * 1024);
        else if (lane < 8) dma16(wx, (u32)(lane * 16), L_WX + (j > 16 ? 128u : 0u));
      }
      if (!rare) {
        setup_window(W, lane, nn, fn, sbeg);
        const u32x4 srd = make_srd(ws);
        WAITV(0);
        wg_barrier();
        STAMP(11); CYC(16);
        if (sbeg < send) first_tile(W, stg, srd);
        rare = __builtin_amdgcn_readfirstlane(lds_ld<u32>(L_FLG + 8)) != 0;
        if (!rare) {
          {
            const int nl = etile * 16 + (lane & 15), pi = w * 16 + (lane >> 2);
            const int pn = pi < 120 ? pi / 3 : 63, pc = pi - pn * 3;
            mk = lds_ld<u32>(L_MSK + nl * 4); pk = lds_ld<u32>(L_MSK + pn * 4);
            const u32 mo = L_STG + (u32)((nl & 7) * MROW + 8 * (lane >> 4)) * 4, po = L_STG + PART_P + (u32)((pn & 7) * 48 + pc * 16 + (lane & 3) * 4) * 4;
#pragma unroll
            for (int u = 0; u < 2; ++u) {
              ma[u] = mk ? (u32)__builtin_ctz(mk) * STGW + mo : L_ZERO;
              pa[u] = pk ? (u32)__builtin_ctz(pk) * STGW + po : L_ZERO;
              mk &= mk - 1; pk &= pk - 1;
            }
            asm volatile("" : "+v"(ma[0]), "+v"(ma[1]), "+v"(pa[0]), "+v"(pa[1]), "+v"(mk), "+v"(pk));
          }
          if (sbeg < send) {
            float wx0 = b2f(lds_ld<u16>(L_WX + (lane & 31) * 2)), wx1 = b2f(lds_ld<u16>(L_WX + 64 + (lane & 31) * 2));
            asm volatile("" : "+v"(wx0), "+v"(wx1));
            run_tiles(W, stg, srd, sbeg, send);
            STAMP(12); CYC(17);
            asm volatile("" : "+v"(lane), "+v"(wx0), "+v"(wx1));
            const u32 fa = stg + (u32)((lane >> 5) * MROW + (lane & 31)) * 4, fp = stg + PART_P + (u32)((lane >> 5) * 48 + ((lane & 31) >> 1)) * 4;
            const u32 touched = ((2u << (ln - fn)) - 1u) * 0x101u >> (8 - (fn & 7));
#pragma unroll
            for (int g4 = 0; g4 < 4; ++g4) {
              if ((touched >> (2 * g4)) & 3u) {
                lds_st<float>(fa + g4 * 2 * MROW * 4, W.acc0[4 * g4]);
                lds_st<float>(fa + g4 * 2 * MROW * 4 + 128, W.acc1[4 * g4]);
#pragma unroll
                for (int cc = 0; cc < 3; ++cc) {
                  const float v = W.acc0[4 * g4 + 1 + cc] * wx0 + W.acc1[4 * g4 + 1 + cc] * wx1;
                  lds_st<float>(fp + g4 * 384 + 64 * cc, v + dppf<0xB1>(v));
                }
              }
            }
          }
        }
      }
    }
  }
  if (__builtin_expect(rare, 0)) {
    for (u32 i = w * 64 + lane; i < ACCS / 16; i += NW * 64) st16_dev(ws, WS_ACC + (u32)c * ACCS + i * 16, u32x4{0, 0, 0, 0});
    if (lane == 0) __hip_atomic_fetch_max((LAS u32*)(size_t)(L_FLG + 12), Tw, __ATOMIC_RELAXED, __HIP_MEMORY_SCOPE_WORKGROUP);
#pragma unroll 1
    for (u32 vbase = 0;; vbase += 64) {
      asm volatile("s_waitcnt vmcnt(0)" ::: "memory");
      wg_barrier();
      asm volatile("" : "+v"(lane));
      if (vbase >= (u32)__builtin_amdgcn_readfirstlane(lds_ld<u32>(L_FLG + 12))) break;
      if (w == 0) { lds_st<u32x4>(L_CNT + lane * 16, u32x4{0, 0, 0, 0}); lds_st<u32>(L_CNT + 1024 + lane * 4, 0u); lds_st<u32>(L_FLG + 4, 0u); }
      lds_st<u32>(dsc + lane * 4, 0u);
#pragma unroll 1
      for (int i = 0; i < 5; ++i) pos_piece(w * 5 + i, lane, pos);
      asm volatile("s_waitcnt vmcnt(0)" ::: "memory");
      wg_barrier();
      u32x2 ridx1[1], ps1[2];
      u32x4 pd1[2];
      u32 ro1[1], rrank1[2], ca1[2], a61[2], Tw1, nv1, rbase1, sbeg, send, nstart, total;
      flatten<1>(te, w * SW + lane, lane, c, dsc, vbase, ro1, Tw1, nv1, rbase1);
      fetch_index<1>(ws, ro1, ridx1);
      fetch_attrs<1>(lane, ws, ro1, nv1, rbase1);
      WAITV(0); asm volatile("" : "+v"(ridx1[0]));
      rank_records<1>(lane, b, ridx1, rrank1, ca1, a61, ps1, pd1);
      wg_barrier();
      scan_nodes(lane, nstart, total);
      scatter_records<1>(L_EA - POFF + (rbase1 + 2 * lane) * 8, ridx1, rrank1, ca1, a61, ps1, pd1);
      finish_lists(lane, w, nstart, total, sbeg, send);
      wg_barrier();
#pragma unroll 1
      while (sbeg < send) {
        asm volatile("" : "+v"(lane));
        const int base = __builtin_popcountll(__ballot(nstart <= sbeg && lane < nn)) - 1;
        const u32 wend = umin(__builtin_amdgcn_readlane(nstart, base + 8 < nn ? base + 8 : nn), send);
        Win V;
        const u32x4 srd = make_srd(ws);
        init_window(V, lane, stg);
        setup_window(V, lane, nn, base, sbeg);
        first_tile(V, stg, srd);
        run_tiles(V, stg, srd, sbeg, wend);
        asm volatile("" : "+v"(lane));
        const float wx0 = b2f(lds_ld<u16>(L_WX + (lane & 31) * 2)), wx1 = b2f(lds_ld<u16>(L_WX + 64 + (lane & 31) * 2));
        const int lastn = __builtin_popcountll(__ballot(nstart < wend && lane < nn)) - 1;
        GAS float* ga = (GAS float*)(ws + WS_ACC + (u32)c * ACCS);
#pragma unroll
        for (int g4 = 0; g4 < 4; ++g4) {
          float pv[3];
#pragma unroll
          for (int cc = 0; cc < 3; ++cc) {
            const float v = V.acc0[4 * g4 + 1 + cc] * wx0 + V.acc1[4 * g4 + 1 + cc] * wx1;
            pv[cc] = v + dppf<0xB1>(v);
          }
          const int nd = base + ((2 * g4 + (lane >> 5) - base) & 7);
          if (nd <= lastn) {
            __hip_atomic_fetch_add(ga + nd * 112 + (lane & 31), V.acc0[4 * g4], RLX_AGENT);
            __hip_atomic_fetch_add(ga + nd * 112 + 32 + (lane & 31), V.acc1[4 * g4], RLX_AGENT);
            if (!(lane & 1)) {
#pragma unroll
              for (int cc = 0; cc < 3; ++cc) __hip_atomic_fetch_add(ga + nd * 112 + 64 + 16 * cc + ((lane & 31) >> 1), pv[cc], RLX_AGENT);
            }
          }
        }
        sbeg = wend;
      }
    }
    ma[0] = ma[1] = pa[0] = pa[1] = L_ZERO; mk = pk = 0;
  }

  f32x4 z;
  bf16x8 wh2[2];
  u32x2 hr;
  asm volatile("" : "+v"(lane));
  node_operands(lane, etile, eT, z, wh2, hr);
  asm volatile("" : "+v"(z), "+v"(wh2[0]), "+v"(wh2[1]), "+v"(hr));
  STAMP(13);
  wg_barrier();
  STAMP(14);
  {
    f32x4 mm[4], pp;
    mm[0] = lds_ld<f32x4>(ma[0]) + lds_ld<f32x4>(ma[1]); mm[1] = lds_ld<f32x4>(ma[0] + 16) + lds_ld<f32x4>(ma[1] + 16);
    mm[2] = lds_ld<f32x4>(ma[0] + 128) + lds_ld<f32x4>(ma[1] + 128); mm[3] = lds_ld<f32x4>(ma[0] + 144) + lds_ld<f32x4>(ma[1] + 144);
    pp = lds_ld<f32x4>(pa[0]) + lds_ld<f32x4>(pa[1]);
    asm volatile("" : "+v"(lane));
    const int g = lane >> 4, nl = etile * 16 + (lane & 15), pi = w * 16 + (lane >> 2);
    const bool nv2 = nl < nn, pvld = pi < nn * 3;
    if (__ballot(mk | pk) || rare) {
      const int pn = pvld ? pi / 3 : 0, pc = pvld ? pi - pn * 3 : 0;
      const u32 mo = L_STG + (u32)((nl & 7) * MROW + 8 * g) * 4, po = L_STG + PART_P + (u32)((pn & 7) * 48 + pc * 16 + (lane & 3) * 4) * 4;
      while (__ballot(mk | pk)) {
        const u32 m2 = mk ? (u32)__builtin_ctz(mk) * STGW + mo : L_ZERO, p2 = pk ? (u32)__builtin_ctz(pk) * STGW + po : L_ZERO;
        mk &= mk - 1; pk &= pk - 1;
        mm[0] += lds_ld<f32x4>(m2); mm[1] += lds_ld<f32x4>(m2 + 16); mm[2] += lds_ld<f32x4>(m2 + 128); mm[3] += lds_ld<f32x4>(m2 + 144);
        pp += lds_ld<f32x4>(p2);
      }
      if (rare) {
        GAS float* ga = (GAS float*)(ws + WS_ACC + (u32)c * ACCS);
#pragma unroll
        for (int i = 0; i < 4; ++i) {
          mm[0][i] += __hip_atomic_load(ga + (nv2 ? nl : 0) * 112 + 8 * g + i, RLX_AGENT);
          mm[1][i] += __hip_atomic_load(ga + (nv2 ? nl : 0) * 112 + 8 * g + 4 + i, RLX_AGENT);
          mm[2][i] += __hip_atomic_load(ga + (nv2 ? nl : 0) * 112 + 32 + 8 * g + i, RLX_AGENT);
          mm[3][i] += __hip_atomic_load(ga + (nv2 ? nl : 0) * 112 + 32 + 8 * g + 4 + i, RLX_AGENT);
          pp[0] += __hip_atomic_load(ga + pn * 112 + 64 + pc * 16 + (lane & 3) * 4 + i, RLX_AGENT);
        }
      }
    }
    f32x4 zm = {0.f, 0.f, 0.f, 0.f};
#pragma unroll
    for (int ks = 0; ks < 2; ++ks) {
      const f32x4 a = mm[2 * ks], cc = mm[2 * ks + 1];
      zm = MFMA16(wh2[ks], as_frag(u32x4{pk2(a[0], a[1]), pk2(a[2], a[3]), pk2(cc[0], cc[1]), pk2(cc[2], cc[3])}), zm);
    }
    float ps = (pp[0] + pp[1]) + (pp[2] + pp[3]);
    ps += dppf<0xB1>(ps);
    ps += dppf<0x4E>(ps);
    if (pvld && (lane & 3) == 0) out[NN * 64 + n0 * 3 + pi] = (u16)(pk2(b2f(pold) + ps * NLN2, 0.f) & 0xffffu);
    float o[4];
#pragma unroll
    for (int i = 0; i < 4; ++i) { const float zz = z[i] + zm[i] * NLN2; o[i] = zz * __builtin_amdgcn_rcpf(1.f + __builtin_amdgcn_exp2f(zz * NLOG2E)); }
    if (nv2) {
      const u32x2 ov = {pk2(b2f(hr[0]) + o[0], bhi(hr[0]) + o[1]), pk2(b2f(hr[1]) + o[2], bhi(hr[1]) + o[3])};
      *(GAS u32x2*)(out + (n0 + nl) * 64 + 16 * eT + 4 * g) = ov;
    }
  }
}

extern "C" void kernel_launch(void* const* d_in, const int* in_sizes, int n_in, void* d_out, int out_size, void* d_ws, size_t ws_size,
                              hipStream_t stream) {
  (void)hipFuncSetAttribute((const void*)egnn_fused, hipFuncAttributeMaxDynamicSharedMemorySize, (int)L_END);
  egnn_fused<<<NBK, NW * 64, L_END, stream>>>((const u16*)d_in[0], (const u16*)d_in[1], d_in[2], d_in[3],
                                              d_in[4], (const u16*)d_in[5], (const u16*)d_in[6], (const u16*)d_in[7],
                                              (char*)d_ws + WS_BASE, (u16*)d_out);
}
```

```cpp
#include <hip/hip_runtime.h>
#include <hip/hip_bf16.h>
#include <stdint.h>

typedef unsigned short u16;
typedef unsigned int u32;
typedef unsigned long long u64;
typedef __attribute__((ext_vector_type(16))) float f32x16;
typedef __attribute__((ext_vector_type(4))) float f32x4;
typedef __attribute__((ext_vector_type(2))) float f32x2;
typedef __attribute__((ext_vector_type(8))) __bf16 bf16x8;
typedef __attribute__((ext_vector_type(2))) __bf16 bf16x2;
typedef __attribute__((ext_vector_type(4))) short s16x4;
typedef __attribute__((ext_vector_type(4))) u32 u32x4;
typedef __attribute__((ext_vector_type(2))) u32 u32x2;
typedef __attribute__((ext_vector_type(8))) u64 u64x8;
typedef __attribute__((ext_vector_type(2))) u64 u64x2;
#define LAS __attribute__((address_space(3)))
#define GAS __attribute__((address_space(1)))
#define DI __device__ __forceinline__
#define RLX_AGENT __ATOMIC_RELAXED, __HIP_MEMORY_SCOPE_AGENT

constexpr int NN = 10000, NBK = 256, CH = 2500, CHS = 2560;
constexpr float NLOG2E = -1.4426950408889634f, NLN2 = -0.6931471805599453f;

#define WS_BASE 0
constexpr u32 WS_IDX = 0;
constexpr u32 WS_EA  = WS_IDX + NBK * CHS * 4;
constexpr u32 WS_TAB = WS_EA + NBK * CHS * 8;
constexpr u32 WS_FLE = WS_TAB + NBK * NBK * 8;
constexpr u32 WS_BP  = WS_FLE + NBK * 1024;
constexpr u32 WS_EP  = WS_BP + NN * 128;
constexpr u32 WS_ACC = WS_EP + 256;
constexpr u32 ACCS = 40 * 112 * 4;
constexpr u32 WS_END = WS_ACC + NBK * ACCS;

#define STAMP_DECL
#define STAMP(i)
#define STAMP_OUT()
#define CYC(i)

DI float b2f(u32 x) { return __uint_as_float(x << 16); }
DI float bhi(u32 x) { return __uint_as_float(x & 0xffff0000u); }
DI u32 pk2(float a, float b) { f32x2 v = {a, b}; return __builtin_bit_cast(u32, __builtin_convertvector(v, bf16x2)); }
DI float rbf(float a) { return b2f(pk2(a, 0.f) & 0xffffu); }
DI int first_node(int b) { return (b * 625 + 15) >> 4; }

template <int CTRL, int RM> DI u32 dpp0(u32 x) { return (u32)__builtin_amdgcn_update_dpp(0, (int)x, CTRL, RM, 0xf, false); }
DI u32 scan_add(u32 x) {
  x += dpp0<0x111, 0xf>(x); x += dpp0<0x112, 0xf>(x); x += dpp0<0x114, 0xf>(x); x += dpp0<0x118, 0xf>(x);
  x += dpp0<0x142, 0xa>(x); x += dpp0<0x143, 0xc>(x); return x;
}
DI u32 umax(u32 a, u32 b) { return a > b ? a : b; }
DI u32 umin(u32 a, u32 b) { return a < b ? a : b; }
DI u32 scan_max(u32 x) {
  x = umax(x, dpp0<0x111, 0xf>(x)); x = umax(x, dpp0<0x112, 0xf>(x)); x = umax(x, dpp0<0x114, 0xf>(x)); x = umax(x, dpp0<0x118, 0xf>(x));
  x = umax(x, dpp0<0x142, 0xa>(x)); x = umax(x, dpp0<0x143, 0xc>(x)); return x;
}
template <int CTRL> DI float dppf(float v) { return __uint_as_float((u32)__builtin_amdgcn_update_dpp(0, (int)__float_as_uint(v), CTRL, 0xf, 0xf, false)); }
DI u32x2 tr_read(u32 addr) { return __builtin_bit_cast(u32x2, __builtin_amdgcn_ds_read_tr16_b64_v4i16((LAS s16x4*)(size_t)addr)); }
template <typename T> DI T lds_ld(u32 addr) { return *(LAS T*)(size_t)addr; }
template <typename T> DI void lds_st(u32 addr, T v) { *(LAS T*)(size_t)addr = v; }
DI u32 lds_add(u32 addr, u32 v) { return __hip_atomic_fetch_add((LAS u32*)(size_t)addr, v, __ATOMIC_RELAXED, __HIP_MEMORY_SCOPE_WORKGROUP); }
DI int med3(int a, int lo, int hi) { int r; asm("v_med3_i32 %0, %1, %2, %3" : "=v"(r) : "v"(a), "v"(lo), "v"(hi)); return r; }
DI void dma16(const GAS void* base, u32 voff, u32 ldsaddr) {
  asm volatile("s_mov_b32 m0, %2\n\ts_nop 0\n\tglobal_load_lds_dwordx4 %0, %1" ::"v"(voff), "s"(base), "s"(ldsaddr) : "memory");
}
DI void dma16_dev(const GAS void* base, u32 voff, u32 ldsaddr) {
  asm volatile("s_mov_b32 m0, %2\n\ts_nop 0\n\tglobal_load_lds_dwordx4 %0, %1 sc1" ::"v"(voff), "s"(base), "s"(ldsaddr) : "memory");
}
DI void st16_dev(const GAS void* base, u32 voff, u32x4 v) {
  asm volatile("global_store_dwordx4 %0, %1, %2 sc1\n\ts_nop 1" ::"v"(voff), "v"(v), "s"(base) : "memory");
}
#define WAITV(n) WAITV_(n)
#define WAITV_(n) do { asm volatile("s_waitcnt vmcnt(" #n ")" ::: "memory"); __builtin_amdgcn_sched_barrier(0); } while (0)
DI void wg_barrier() { asm volatile("s_waitcnt lgkmcnt(0)\n\ts_barrier" ::: "memory"); }
DI bf16x8 as_frag(u32x4 v) { return __builtin_bit_cast(bf16x8, v); }
DI bf16x8 as_frag(u32x2 a, u32x2 b) { return __builtin_bit_cast(bf16x8, u32x4{a[0], a[1], b[0], b[1]}); }

#define TMODE 12
#define EDELAY WAITV(0)
#define BPF 1
#define CODE_LINES 138
constexpr int NW = 12, SW = (NBK + NW - 1) / NW, PASSES = 3, CAP = 2880, RCAP = 3200, QFAST = PASSES * 64;
constexpr int STGS = 1088, STGW = 4 * STGS, MROW = 68;
constexpr u32 POFF = 244;
constexpr u32 L_WX = 0;
constexpr u32 L_EA = 256;
constexpr u32 L_OH = L_EA + RCAP * 8;
constexpr u32 L_ZERO = L_OH + 256;
constexpr u32 L_SINK = L_ZERO + 1024;
constexpr u32 L_CNT = L_ZERO + 3328;
constexpr u32 L_MSK = L_CNT + 1280;
constexpr u32 L_FLG = L_MSK + 256;
constexpr u32 L_NST = L_FLG + 64;
constexpr u32 L_PD = L_NST + 1280;
constexpr u32 L_G = 33024;
constexpr u32 L_HR = L_G + 56 * 128;
constexpr u32 L_STG = L_HR + 6144;
constexpr u32 L_WH = L_STG + NW * STGW;
constexpr u32 L_S = L_WH + 16384;
constexpr u32 L_DV = L_S + (CAP + 144) * 8;
constexpr u32 L_END = L_DV + (CAP + 208) * 8;
constexpr u32 PART_P = 8 * MROW * 4;
constexpr u32 L_SEA = L_EA, L_HIST = L_SEA + CHS * 8, L_OFFS = L_HIST + 1024;
constexpr u32 L_POS = L_STG, L_SIDX = L_S, L_WE = L_SIDX + CHS * 4, L_D = L_WE + 16384, DSTR = QFAST * 4;
constexpr u32 SY0 = L_FLG + 16, SY1A = L_FLG + 20, SY1B = L_FLG + 24, SY2 = L_FLG + 28, SYH = L_FLG + 32, SY5 = L_FLG + 36;
static_assert(L_ZERO % 256 == 0 && L_STG % 256 == 0 && L_END <= 163840 && L_PD + 640 <= L_G && L_ZERO + 16 < 0x7F80, "lds layout");
static_assert(64 * NW * 2 <= CAP && CAP % 32 == 0 && L_OFFS + 1024 <= L_OH && L_D + NW * DSTR <= L_END && L_POS + 61440 <= L_S, "aliases");
constexpr u32 SPIN_CAP = 1u << 22;
#define MFMA16(a, b, c) __builtin_amdgcn_mfma_f32_16x16x32_bf16(a, b, c, 0, 0, 0)
#define LIKELY(x) __builtin_expect(!!(x), 1)
#define UNLIKELY(x) __builtin_expect(!!(x), 0)

DI void arrive(u32 addr) {
  asm volatile("s_mov_b64 exec, 1\n\tds_add_u32 %0, %1\n\ts_mov_b64 exec, -1" ::"v"(addr), "v"(1u) : "memory");
}
DI u32 ticket(u32 addr, u32 v) {
  u32 r, t;
  asm volatile("s_mov_b64 exec, 1\n\tds_add_rtn_u32 %1, %2, %3\n\ts_mov_b64 exec, -1\n\ts_waitcnt lgkmcnt(0)\n\tv_readfirstlane_b32 %0, %1\n\ts_nop 3"
               : "=s"(r), "=&v"(t) : "v"(addr), "v"(v) : "memory");
  return r;
}
DI void await(u32 addr, u32 n) {
  while ((u32)__builtin_amdgcn_readfirstlane(__hip_atomic_load((LAS u32*)(size_t)addr, __ATOMIC_RELAXED, __HIP_MEMORY_SCOPE_WORKGROUP)) < n) {}
  asm volatile("" ::: "memory");
}
DI u32x4 make_srd(u64 base, u32 stride, u32 n) { return u32x4{(u32)base, (u32)(base >> 32) | (stride << 16), n, 0x00020000u}; }
DI void pos_pieces(u32x4 srd, u32 voff, u32 first) {
  asm volatile("s_mov_b32 m0, %2\n\ts_nop 0\n\tbuffer_load_dwordx4 %0, %1, 0 offen lds\n\tbuffer_load_dwordx4 %0, %1, 0 offen offset:1024 lds\n\t"
               "buffer_load_dwordx4 %0, %1, 0 offen offset:2048 lds\n\tbuffer_load_dwordx4 %0, %1, 0 offen offset:3072 lds\n\t"
               "s_add_u32 m0, %2, 1024\n\ts_nop 0\n\tbuffer_load_dwordx4 %3, %1, 0 offen offset:3072 lds"
               ::"v"(voff), "s"(srd), "s"(L_POS + first), "v"(voff + 1024) : "memory", "scc");
}
DI u64 poll64(const GAS char* base, u32 voff) {
  u64 g;
  asm volatile("global_load_dwordx2 %0, %1, %2 sc1\n\ts_waitcnt vmcnt(0)" : "=&v"(g) : "v"(voff), "s"(base) : "memory");
  return g;
}

template <int P>
DI void flatten(u32 te, u32 segbase, int lane, u32 padoff, u32 dsc, u32 vbase, u32 (&ro)[P], u64 (&on)[P], u32& Tw, u32& nv) {
  const u32 scnt = ((te >> 12) + 1) >> 1;
  const u32 sincl = scan_add(scnt);
  const u32 sexcl = sincl - scnt;
  Tw = __builtin_amdgcn_readlane(sincl, 63);
  const u32 delta = segbase + (te & 0xfffu) * 8 - 16 * sexcl;
  if (scnt > 0 && sexcl < vbase + 64 * P) {
    const u32 pp = sexcl > vbase ? sexcl - vbase : 0u;
    __hip_atomic_fetch_max((LAS u32*)(size_t)(dsc + pp * 4), delta, __ATOMIC_RELAXED, __HIP_MEMORY_SCOPE_WAVEFRONT);
  }
  nv = Tw > vbase ? umin(Tw - vbase, 64 * P) : 0u;
  asm volatile("" ::: "memory");
  u32 carry = 0;
#pragma unroll
  for (int k = 0; k < P; ++k) ro[k] = lds_ld<u32>(dsc + (64 * k + lane) * 4);
#pragma unroll
  for (int k = 0; k < P; ++k) {
    const u32 dl = umax(scan_max(ro[k]), carry);
    carry = __builtin_amdgcn_readlane(dl, 63);
    const bool act = 64 * k + lane < nv;
    on[k] = __ballot(act);
    ro[k] = act ? 16 * (vbase + 64 * k + lane) + dl : padoff;
  }
}
template <int P>
DI void fetch_index(const GAS char* ws, const u32 (&ro)[P], u32x2 (&ridx)[P]) {
#pragma unroll
  for (int k = 0; k < P; ++k) asm volatile("global_load_dwordx2 %0, %1, %2 sc1" : "=&v"(ridx[k]) : "v"(ro[k] >> 1), "s"(ws + WS_IDX) : "memory");
}
template <int P>
DI void fetch_attrs(const GAS char* ws, const u32 (&ro)[P], const u64 (&on)[P], u32 rbase) {
#pragma unroll
  for (int k = 0; k < P; ++k)
    asm volatile("s_mov_b32 m0, %2\n\ts_mov_b64 exec, %3\n\ts_cbranch_execz 1f\n\tglobal_load_lds_dwordx4 %0, %1 sc1\n1:\n\ts_mov_b64 exec, -1"
                 ::"v"(ro[k]), "s"(ws + WS_EA), "s"(L_EA + (rbase + 128 * k) * 8), "s"(on[k]) : "memory");
}
template <int K>
DI void rank_records(u32 rep4, u32 dummy, u32 bkey, const u32x2* ridx, u32 (&rrank)[2 * K], u32 (&ca)[2 * K]) {
#pragma unroll
  for (int i = 0; i < 2 * K; ++i) {
    const u32 x = ridx[i >> 1][i & 1];
    ca[i] = (x ^ bkey) < (40u << 18) ? ((x >> 14) & 0x3f0u) | rep4 : dummy;
    rrank[i] = lds_add(L_CNT + ca[i], 1u);
  }
}
template <int K>
DI void geometry(u32 eptr, const u32x2* ridx, const u32 (&ca)[2 * K], u32x2 (&sent)[2 * K], u32x2 (&rdv)[2 * K]) {
  u32 a6[2 * K];
  u32x2 ps[2 * K];
  u32x4 pd[2 * K];
#pragma unroll
  for (int i = 0; i < 2 * K; ++i) {
    a6[i] = (ridx[i >> 1][i & 1] & 0xffffu) * 6 + L_POS;
    const u32 pa = a6[i] & ~3u;
    ps[i] = u32x2{lds_ld<u32>(pa), lds_ld<u32>(pa + 4)};
    pd[i] = lds_ld<u32x4>(L_PD + (ca[i] & 0x3f0u));
  }
  __builtin_amdgcn_sched_barrier(0);
#pragma unroll
  for (int i = 0; i < 2 * K; ++i) {
    const u32 x = ridx[i >> 1][i & 1];
    const u32 sxy = __builtin_amdgcn_alignbyte(ps[i][1], ps[i][0], a6[i]), sz = __builtin_amdgcn_alignbyte(ps[i][1], ps[i][1], a6[i]);
    const float dx = __uint_as_float(pd[i][0]) - b2f(sxy), dy = __uint_as_float(pd[i][1]) - bhi(sxy), dz = __uint_as_float(pd[i][2]) - b2f(sz);
    const float d2 = (dx * dx + dy * dy + dz * dz) * NLOG2E;
    sent[i] = u32x2{(x & 0xffffu) | (pk2(d2, d2) & 0xffff0000u), eptr + (u32)((i >> 1) * 1024 + (i & 1) * 8) + pd[i][3]};
    rdv[i] = u32x2{pk2(1.0f, dx), pk2(dy, dz)};
    asm volatile("" : "+v"(sent[i]), "+v"(rdv[i]));
  }
}
template <int K>
DI void scatter_records(const u32 (&rrank)[2 * K], const u32 (&ca)[2 * K], const u32x2 (&sent)[2 * K], const u32x2 (&rdv)[2 * K]) {
  u32 st[2 * K];
#pragma unroll
  for (int i = 0; i < 2 * K; ++i) st[i] = lds_ld<u32>(L_NST + ca[i]);
#pragma unroll
  for (int i = 0; i < 2 * K; ++i) {
    const u32 sa = st[i] + rrank[i] * 8;
    lds_st<u32x2>(sa, sent[i]);
    lds_st<u32x2>(sa + ((ca[i] & 0x3f0u) >> 1) + (L_DV - L_S + 8), rdv[i]);
  }
}
DI void scan_nodes(u32x4 cn, int lane, u32& nstart, u32& total) {
  const u32 tot = cn[0] + cn[1] + cn[2] + cn[3];
  const u32 incl = scan_add(tot);
  nstart = incl - tot;
  total = __builtin_amdgcn_readlane(incl, 63);
  const u32 a = L_S + nstart * 8;
  lds_st<u32x4>(L_NST + lane * 16, u32x4{a, a + cn[0] * 8, a + (cn[0] + cn[1]) * 8, a + (tot - cn[3]) * 8});
  asm volatile("" ::: "memory");
}
DI void finish_lists(int lane, int w, u32 nstart, u32 total, u32& sbeg, u32& send) {
  if (w == NW - 1) {
    lds_st<u32x2>(L_DV + (nstart + lane) * 8, u32x2{0, 0});
    lds_st<u32x2>(L_S + (total + lane) * 8, u32x2{0u, (L_ZERO - POFF) * 0x10001u});
  }
  const int ntiles = (int)(total + 31) >> 5;
  const int tq = ntiles / NW, tr = ntiles - tq * NW;
  const int tb = tq * w + (w < tr ? w : tr), tend = tb + tq + (w < tr ? 1 : 0);
  sbeg = (u32)tb * 32;
  send = umin((u32)tend * 32, total);
}

#define TILE_FIRST \
  "ds_read_u16 v92, %[s4a] offset:256\n\t" \
  "ds_read_u16 v94, %[s4a] offset:264\n\t" \
  "ds_read_u16 v96, %[s4a] offset:272\n\t" \
  "ds_read_u16 v98, %[s4a] offset:280\n\t" \
  "v_mov_b32 v93, %[lp]\n\t" \
  "v_mov_b32 v95, %[lp]\n\t" \
  "v_mov_b32 v97, %[lp]\n\t" \
  "v_mov_b32 v99, %[lp]\n\t" \
  "s_waitcnt lgkmcnt(0)\n\t" \
  "s_mov_b32 m0, %[stg]\n\t" \
  "s_nop 0\n\t" \
  "buffer_load_dwordx4 v[92:93], %[srd], 0 idxen offen sc1 lds\n\t" \
  "s_add_u32 m0, %[stg], 1088\n\t" \
  "s_nop 0\n\t" \
  "buffer_load_dwordx4 v[94:95], %[srd], 0 idxen offen sc1 lds\n\t" \
  "s_add_u32 m0, %[stg], 2176\n\t" \
  "s_nop 0\n\t" \
  "buffer_load_dwordx4 v[96:97], %[srd], 0 idxen offen sc1 lds\n\t" \
  "s_add_u32 m0, %[stg], 3264\n\t" \
  "s_nop 0\n\t" \
  "buffer_load_dwordx4 v[98:99], %[srd], 0 idxen offen sc1 lds\n\t"
#define TILE_FIRST_CLOBBERS "v92", "v93", "v94", "v95", "v96", "v97", "v98", "v99"
#define TILE_LOOP \
  "ds_read_u16 v100, %[s1a] offset:256\n\t" \
  "v_mov_b32 v93, %[lp]\n\t" \
  "v_mov_b32 v95, %[lp]\n\t" \
  "v_mov_b32 v97, %[lp]\n\t" \
  "v_mov_b32 v99, %[lp]\n\t" \
  "v_subrev_u32 v101, 64, %[lo]\n\t" \
  "v_subrev_u32 v102, 64, %[hi]\n\t" \
  "v_subrev_u32 v103, 128, %[lo]\n\t" \
  "v_subrev_u32 v104, 128, %[hi]\n\t" \
  "v_subrev_u32 v105, 192, %[lo]\n\t" \
  "v_subrev_u32 v106, 192, %[hi]\n\t" \
  "s_waitcnt lgkmcnt(0)\n\t" \
  "v_bfi_b32 v64, %[mh], v100, %[s1a]\n\t" \
  "ds_read_b64 v[88:89], v100 offset:244\n\t" \
  "ds_read_b64 v[90:91], v64 offset:252\n\t" \
  "ds_read_u16 v100, %[s1a] offset:512\n\t" \
  "ds_read_u16 v92, %[s4a] offset:512\n\t" \
  "ds_read_u16 v94, %[s4a] offset:520\n\t" \
  "ds_read_u16 v96, %[s4a] offset:528\n\t" \
  "ds_read_u16 v98, %[s4a] offset:536\n\t" \
  "v_add_u32 %[s1a], 0x100, %[s1a]\n\t" \
  "v_add_u32 %[s4a], 0x100, %[s4a]\n\t" \
  "1:\n\t" \
  "s_setprio 3\n\t" \
  "v_med3_i32 v64, %[da], %[lo], %[hi]\n\t" \
  "v_med3_i32 v65, %[da], v101, v102\n\t" \
  "v_med3_i32 v66, %[da], v103, v104\n\t" \
  "v_med3_i32 v67, %[da], v105, v106\n\t" \
  "ds_read_b64_tr_b16 v[80:81], v64 offset:0\n\t" \
  "ds_read_b64_tr_b16 v[82:83], v65 offset:64\n\t" \
  "ds_read_b64_tr_b16 v[84:85], v66 offset:128\n\t" \
  "ds_read_b64_tr_b16 v[86:87], v67 offset:192\n\t" \
  "v_add_u32 %[da], 0x100, %[da]\n\t" \
  "s_waitcnt vmcnt(0)\n\t" \
  "ds_read_b64_tr_b16 v[32:33], %[cb] offset:0\n\t" \
  "ds_read_b64_tr_b16 v[34:35], %[cb] offset:2176\n\t" \
  "ds_read_b64_tr_b16 v[36:37], %[cb] offset:256\n\t" \
  "ds_read_b64_tr_b16 v[38:39], %[cb] offset:2432\n\t" \
  "ds_read_b64_tr_b16 v[40:41], %[cb] offset:512\n\t" \
  "ds_read_b64_tr_b16 v[42:43], %[cb] offset:2688\n\t" \
  "ds_read_b64_tr_b16 v[44:45], %[cb] offset:768\n\t" \
  "ds_read_b64_tr_b16 v[46:47], %[cb] offset:2944\n\t" \
  "ds_read_b64_tr_b16 v[48:49], %[cb] offset:64\n\t" \
  "ds_read_b64_tr_b16 v[50:51], %[cb] offset:2240\n\t" \
  "ds_read_b64_tr_b16 v[52:53], %[cb] offset:320\n\t" \
  "ds_read_b64_tr_b16 v[54:55], %[cb] offset:2496\n\t" \
  "ds_read_b64_tr_b16 v[56:57], %[cb] offset:576\n\t" \
  "ds_read_b64_tr_b16 v[58:59], %[cb] offset:2752\n\t" \
  "ds_read_b64_tr_b16 v[60:61], %[cb] offset:832\n\t" \
  "ds_read_b64_tr_b16 v[62:63], %[cb] offset:3008\n\t" \
  "s_waitcnt lgkmcnt(8)\n\t" \
  "v_mfma_f32_32x32x16_bf16 v[32:47], v[88:91], %[wf0], v[32:47]\n\t" \
  "s_sub_u32 %[rem], %[rem], 1\n\t" \
  "s_cmp_eq_u32 %[rem], 0\n\t" \
  "s_waitcnt lgkmcnt(0)\n\t" \
  "v_mfma_f32_32x32x16_bf16 v[48:63], v[88:91], %[wf1], v[48:63]\n\t" \
  "s_cbranch_scc1 2f\n\t" \
  "s_mov_b32 m0, %[stg]\n\t" \
  "s_nop 0\n\t" \
  "buffer_load_dwordx4 v[92:93], %[srd], 0 idxen offen sc1 lds\n\t" \
  "s_add_u32 m0, %[stg], 1088\n\t" \
  "s_nop 0\n\t" \
  "buffer_load_dwordx4 v[94:95], %[srd], 0 idxen offen sc1 lds\n\t" \
  "s_add_u32 m0, %[stg], 2176\n\t" \
  "s_nop 0\n\t" \
  "buffer_load_dwordx4 v[96:97], %[srd], 0 idxen offen sc1 lds\n\t" \
  "s_add_u32 m0, %[stg], 3264\n\t" \
  "s_nop 0\n\t" \
  "buffer_load_dwordx4 v[98:99], %[srd], 0 idxen offen sc1 lds\n\t" \
  "v_bfi_b32 v64, %[mh], v100, %[s1a]\n\t" \
  "ds_read_b64 v[88:89], v100 offset:244\n\t" \
  "ds_read_b64 v[90:91], v64 offset:252\n\t" \
  "ds_read_u16 v100, %[s1a] offset:512\n\t" \
  "ds_read_u16 v92, %[s4a] offset:512\n\t" \
  "ds_read_u16 v94, %[s4a] offset:520\n\t" \
  "ds_read_u16 v96, %[s4a] offset:528\n\t" \
  "ds_read_u16 v98, %[s4a] offset:536\n\t" \
  "v_add_u32 %[s1a], 0x100, %[s1a]\n\t" \
  "v_add_u32 %[s4a], 0x100, %[s4a]\n\t" \
  "s_setprio 2\n\t" \
  "s_cmp_lg_u32 %[rem], 1\n\t" \
  "s_cbranch_scc1 3f\n\t" \
  "s_setprio 1\n\t" \
  "s_branch 3f\n\t" \
  "2:\n\t" \
  "s_setprio 0\n\t" \
  "s_nop 7\n\t" \
  "3:\n\t" \
  "v_exp_f32 v64, v32\n\t" \
  "v_exp_f32 v65, v33\n\t" \
  "v_exp_f32 v66, v34\n\t" \
  "v_exp_f32 v67, v35\n\t" \
  "v_exp_f32 v68, v36\n\t" \
  "v_exp_f32 v69, v37\n\t" \
  "v_exp_f32 v70, v38\n\t" \
  "v_exp_f32 v71, v39\n\t" \
  "v_exp_f32 v72, v40\n\t" \
  "v_exp_f32 v73, v41\n\t" \
  "v_exp_f32 v74, v42\n\t" \
  "v_exp_f32 v75, v43\n\t" \
  "v_exp_f32 v76, v44\n\t" \
  "v_exp_f32 v77, v45\n\t" \
  "v_exp_f32 v78, v46\n\t" \
  "v_exp_f32 v79, v47\n\t" \
  "v_pk_add_f32 v[64:65], v[64:65], 1.0 op_sel_hi:[1,0]\n\t" \
  "v_pk_add_f32 v[66:67], v[66:67], 1.0 op_sel_hi:[1,0]\n\t" \
  "v_pk_add_f32 v[68:69], v[68:69], 1.0 op_sel_hi:[1,0]\n\t" \
  "v_pk_add_f32 v[70:71], v[70:71], 1.0 op_sel_hi:[1,0]\n\t" \
  "v_pk_add_f32 v[72:73], v[72:73], 1.0 op_sel_hi:[1,0]\n\t" \
  "v_pk_add_f32 v[74:75], v[74:75], 1.0 op_sel_hi:[1,0]\n\t" \
  "v_pk_add_f32 v[76:77], v[76:77], 1.0 op_sel_hi:[1,0]\n\t" \
  "v_pk_add_f32 v[78:79], v[78:79], 1.0 op_sel_hi:[1,0]\n\t" \
  "v_rcp_f32 v64, v64\n\t" \
  "v_rcp_f32 v65, v65\n\t" \
  "v_rcp_f32 v66, v66\n\t" \
  "v_rcp_f32 v67, v67\n\t" \
  "v_rcp_f32 v68, v68\n\t" \
  "v_rcp_f32 v69, v69\n\t" \
  "v_rcp_f32 v70, v70\n\t" \
  "v_rcp_f32 v71, v71\n\t" \
  "v_rcp_f32 v72, v72\n\t" \
  "v_rcp_f32 v73, v73\n\t" \
  "v_rcp_f32 v74, v74\n\t" \
  "v_rcp_f32 v75, v75\n\t" \
  "v_rcp_f32 v76, v76\n\t" \
  "v_rcp_f32 v77, v77\n\t" \
  "v_rcp_f32 v78, v78\n\t" \
  "v_rcp_f32 v79, v79\n\t" \
  "v_pk_mul_f32 v[32:33], v[32:33], v[64:65]\n\t" \
  "v_pk_mul_f32 v[34:35], v[34:35], v[66:67]\n\t" \
  "v_pk_mul_f32 v[36:37], v[36:37], v[68:69]\n\t" \
  "v_pk_mul_f32 v[38:39], v[38:39], v[70:71]\n\t" \
  "v_pk_mul_f32 v[40:41], v[40:41], v[72:73]\n\t" \
  "v_pk_mul_f32 v[42:43], v[42:43], v[74:75]\n\t" \
  "v_pk_mul_f32 v[44:45], v[44:45], v[76:77]\n\t" \
  "v_pk_mul_f32 v[46:47], v[46:47], v[78:79]\n\t" \
  "v_cvt_pk_bf16_f32 v32, v32, v33\n\t" \
  "v_cvt_pk_bf16_f32 v33, v34, v35\n\t" \
  "v_cvt_pk_bf16_f32 v34, v36, v37\n\t" \
  "v_cvt_pk_bf16_f32 v35, v38, v39\n\t" \
  "v_cvt_pk_bf16_f32 v36, v40, v41\n\t" \
  "v_cvt_pk_bf16_f32 v37, v42, v43\n\t" \
  "v_cvt_pk_bf16_f32 v38, v44, v45\n\t" \
  "v_cvt_pk_bf16_f32 v39, v46, v47\n\t" \
  "v_exp_f32 v64, v48\n\t" \
  "v_exp_f32 v65, v49\n\t" \
  "v_mfma_f32_32x32x16_bf16 v[0:15], v[80:83], v[32:35], v[0:15]\n\t" \
  "v_exp_f32 v66, v50\n\t" \
  "v_exp_f32 v67, v51\n\t" \
  "v_exp_f32 v68, v52\n\t" \
  "v_exp_f32 v69, v53\n\t" \
  "v_mfma_f32_32x32x16_bf16 v[0:15], v[84:87], v[36:39], v[0:15]\n\t" \
  "v_exp_f32 v70, v54\n\t" \
  "v_exp_f32 v71, v55\n\t" \
  "v_exp_f32 v72, v56\n\t" \
  "v_exp_f32 v73, v57\n\t" \
  "v_exp_f32 v74, v58\n\t" \
  "v_exp_f32 v75, v59\n\t" \
  "v_exp_f32 v76, v60\n\t" \
  "v_exp_f32 v77, v61\n\t" \
  "v_exp_f32 v78, v62\n\t" \
  "v_exp_f32 v79, v63\n\t" \
  "v_pk_add_f32 v[64:65], v[64:65], 1.0 op_sel_hi:[1,0]\n\t" \
  "v_pk_add_f32 v[66:67], v[66:67], 1.0 op_sel_hi:[1,0]\n\t" \
  "v_pk_add_f32 v[68:69], v[68:69], 1.0 op_sel_hi:[1,0]\n\t" \
  "v_pk_add_f32 v[70:71], v[70:71], 1.0 op_sel_hi:[1,0]\n\t" \
  "v_pk_add_f32 v[72:73], v[72:73], 1.0 op_sel_hi:[1,0]\n\t" \
  "v_pk_add_f32 v[74:75], v[74:75], 1.0 op_sel_hi:[1,0]\n\t" \
  "v_pk_add_f32 v[76:77], v[76:77], 1.0 op_sel_hi:[1,0]\n\t" \
  "v_pk_add_f32 v[78:79], v[78:79], 1.0 op_sel_hi:[1,0]\n\t" \
  "v_rcp_f32 v64, v64\n\t" \
  "v_rcp_f32 v65, v65\n\t" \
  "v_rcp_f32 v66, v66\n\t" \
  "v_rcp_f32 v67, v67\n\t" \
  "v_rcp_f32 v68, v68\n\t" \
  "v_rcp_f32 v69, v69\n\t" \
  "v_rcp_f32 v70, v70\n\t" \
  "v_rcp_f32 v71, v71\n\t" \
  "v_rcp_f32 v72, v72\n\t" \
  "v_rcp_f32 v73, v73\n\t" \
  "v_rcp_f32 v74, v74\n\t" \
  "v_rcp_f32 v75, v75\n\t" \
  "v_rcp_f32 v76, v76\n\t" \
  "v_rcp_f32 v77, v77\n\t" \
  "v_rcp_f32 v78, v78\n\t" \
  "v_rcp_f32 v79, v79\n\t" \
  "v_pk_mul_f32 v[48:49], v[48:49], v[64:65]\n\t" \
  "v_pk_mul_f32 v[50:51], v[50:51], v[66:67]\n\t" \
  "v_pk_mul_f32 v[52:53], v[52:53], v[68:69]\n\t" \
  "v_pk_mul_f32 v[54:55], v[54:55], v[70:71]\n\t" \
  "v_pk_mul_f32 v[56:57], v[56:57], v[72:73]\n\t" \
  "v_pk_mul_f32 v[58:59], v[58:59], v[74:75]\n\t" \
  "v_pk_mul_f32 v[60:61], v[60:61], v[76:77]\n\t" \
  "v_pk_mul_f32 v[62:63], v[62:63], v[78:79]\n\t" \
  "v_cvt_pk_bf16_f32 v48, v48, v49\n\t" \
  "v_cvt_pk_bf16_f32 v49, v50, v51\n\t" \
  "v_cvt_pk_bf16_f32 v50, v52, v53\n\t" \
  "v_cvt_pk_bf16_f32 v51, v54, v55\n\t" \
  "v_cvt_pk_bf16_f32 v52, v56, v57\n\t" \
  "v_cvt_pk_bf16_f32 v53, v58, v59\n\t" \
  "v_cvt_pk_bf16_f32 v54, v60, v61\n\t" \
  "v_cvt_pk_bf16_f32 v55, v62, v63\n\t" \
  "v_mfma_f32_32x32x16_bf16 v[16:31], v[80:83], v[48:51], v[16:31]\n\t" \
  "s_cmp_lg_u32 %[rem], 0\n\t" \
  "v_mfma_f32_32x32x16_bf16 v[16:31], v[84:87], v[52:55], v[16:31]\n\t" \
  "s_cbranch_scc1 1b\n\t" \
  "s_nop 11\n\t"
#define TILE_LOOP_CLOBBERS "v32", "v33", "v34", "v35", "v36", "v37", "v38", "v39", "v40", "v41", "v42", "v43", "v44", "v45", "v46", "v47", "v48", "v49", "v50", "v51", "v52", "v53", "v54", "v55", "v56", "v57", "v58", "v59", "v60", "v61", "v62", "v63", "v64", "v65", "v66", "v67", "v68", "v69", "v70", "v71", "v72", "v73", "v74", "v75", "v76", "v77", "v78", "v79", "v80", "v81", "v82", "v83", "v84", "v85", "v86", "v87", "v88", "v89", "v90", "v91", "v92", "v93", "v94", "v95", "v96", "v97", "v98", "v99", "v100", "v101", "v102", "v103", "v104", "v105", "v106"

struct Win { u32 cbase, s4a, s1a, mh, lp; int lo, hi, da; bf16x8 wf0, wf1; f32x16 acc0, acc1; };
DI void init_window(Win& W, int lane, u32 stg) {
  const int hh = lane >> 5, blk = (lane >> 4) & 1, q = (lane >> 2) & 3, p = lane & 3;
  const u32 real = stg + 128 * hh + 32 * blk + 8 * p + (q >> 1) * STGS, zero = L_ZERO + 128 * (1 - hh);
  W.cbase = (q & 1) ? real : zero;
  W.s4a = L_S - 256 + (u32)(4 * (lane >> 3)) * 8; W.s1a = L_S - 256 + (u32)(lane & 31) * 8 + 4 + 2 * hh;
  W.da = (int)L_DV + 8 * (4 * hh + q + 1);
  W.mh = hh ? ~0u : 0u; W.lp = (lane & 7) * 16;
  asm volatile("v_mov_b64 v[0:1], 0\n\tv_mov_b64 v[2:3], 0\n\tv_mov_b64 v[4:5], 0\n\tv_mov_b64 v[6:7], 0\n\tv_mov_b64 v[8:9], 0\n\tv_mov_b64 v[10:11], 0\n\t"
               "v_mov_b64 v[12:13], 0\n\tv_mov_b64 v[14:15], 0\n\tv_mov_b64 v[16:17], 0\n\tv_mov_b64 v[18:19], 0\n\tv_mov_b64 v[20:21], 0\n\tv_mov_b64 v[22:23], 0\n\t"
               "v_mov_b64 v[24:25], 0\n\tv_mov_b64 v[26:27], 0\n\tv_mov_b64 v[28:29], 0\n\tv_mov_b64 v[30:31], 0"
               : "=&{v[0:15]}"(W.acc0), "=&{v[16:31]}"(W.acc1), "+v"(W.cbase), "+v"(W.s4a), "+v"(W.s1a), "+v"(W.mh), "+v"(W.lp), "+v"(W.da));
}
DI void setup_window(Win& W, int lane, int nn, int base, u32 sbeg) {
  const int blk = (lane >> 4) & 1, q = (lane >> 2) & 3, p = lane & 3;
  const u32 s8 = (sbeg & ~31u) * 8;
  W.s4a += s8; W.s1a += s8;
  const int rb = lane < 32 ? 48 : base;
  const int r0 = rb + ((q - rb) & 7), r1 = rb + ((4 + q - rb) & 7);
  const u32 a0 = L_G + r0 * 128 + 32 * blk + 8 * p, a1 = L_G + r1 * 128 + 32 * blk + 8 * p;
  W.wf0 = as_frag(tr_read(a0), tr_read(a1)); W.wf1 = as_frag(tr_read(a0 + 64), tr_read(a1 + 64));
  int np = base + ((4 * blk + p - base) & 7); np = np < nn ? np : nn;
  W.lo = (int)(L_DV - L_S) + (int)lds_ld<u32>(L_NST + np * 16) + 8 * np;
  W.hi = (int)(L_DV - L_S + 8) + (int)lds_ld<u32>(L_NST + np * 16 + 16) + 8 * np;
  W.da += (int)s8 + 8 * np;
  asm volatile("" : "+v"(W.s4a), "+v"(W.s1a), "+v"(W.lo), "+v"(W.hi), "+v"(W.da), "+v"(W.wf0), "+v"(W.wf1));
}
DI void first_tile(const Win& W, u32 stg, u32x4 srd) {
  asm volatile(TILE_FIRST ::[s4a] "v"(W.s4a), [lp] "v"(W.lp), [srd] "s"(srd), [stg] "s"(stg) : "memory", "scc", TILE_FIRST_CLOBBERS);
}
DI void run_tiles(Win& W, u32 stg, u32x4 srd, u32 sbeg, u32 wend) {
  u32 rem = ((wend + 31) >> 5) - (sbeg >> 5);
  asm volatile(TILE_LOOP
               : "+{v[0:15]}"(W.acc0), "+{v[16:31]}"(W.acc1), [rem] "+s"(rem), [s4a] "+v"(W.s4a), [s1a] "+v"(W.s1a), [da] "+v"(W.da)
               : [srd] "s"(srd), [stg] "s"(stg), [cb] "v"(W.cbase), [lp] "v"(W.lp), [mh] "v"(W.mh), [lo] "v"(W.lo), [hi] "v"(W.hi), [wf0] "v"(W.wf0), [wf1] "v"(W.wf1)
               : "memory", "scc", TILE_LOOP_CLOBBERS);
}
DI void load_wh(const GAS u16* wh, u32 voff, u32 dst) {
  asm volatile("s_mov_b32 m0, %2\n\ts_nop 0\n\tglobal_load_lds_dwordx4 %0, %1\n\ts_add_u32 m0, %2, 1024\n\ts_nop 0\n\tglobal_load_lds_dwordx4 %3, %1\n\t"
               "s_add_u32 m0, %2, 2048\n\ts_nop 0\n\tglobal_load_lds_dwordx4 %4, %1\n\ts_add_u32 m0, %2, 3072\n\ts_nop 0\n\tglobal_load_lds_dwordx4 %5, %1"
               ::"v"(voff), "s"(wh), "s"(dst), "v"(voff + 4096), "v"(voff + 8192), "v"(voff + 12288) : "memory", "scc");
}
struct Node { f32x4 z; bf16x8 wh2[2]; u32x2 hr; float pold; };
DI void node_operands(Node& nd, u32 ha, u32 hra, u32 wa, u32 pda) {
  const u32x4 hf0 = lds_ld<u32x4>(ha), hf1 = lds_ld<u32x4>(ha + 64);
  const bf16x8 w0 = as_frag(tr_read(wa), tr_read(wa + 128)), w1 = as_frag(tr_read(wa + 1024), tr_read(wa + 1152));
  nd.wh2[0] = as_frag(tr_read(wa + 2048), tr_read(wa + 2176));
  nd.wh2[1] = as_frag(tr_read(wa + 3072), tr_read(wa + 3200));
  nd.hr = lds_ld<u32x2>(hra);
  nd.pold = lds_ld<float>(pda);
  nd.z = f32x4{0.f, 0.f, 0.f, 0.f};
  nd.z = MFMA16(w0, as_frag(hf0), nd.z);
  nd.z = MFMA16(w1, as_frag(hf1), nd.z);
  asm volatile("" : "+v"(nd.z), "+v"(nd.wh2[0]), "+v"(nd.wh2[1]), "+v"(nd.hr), "+v"(nd.pold));
}
DI void node_update(const Node& nd, const f32x4 (&mm)[4], f32x4 pp, u32x4 osrd, u32 oh, u32 op) {
  f32x4 zm = {0.f, 0.f, 0.f, 0.f};
#pragma unroll
  for (int ks = 0; ks < 2; ++ks) {
    const f32x4 a = mm[2 * ks], cc = mm[2 * ks + 1];
    zm = MFMA16(nd.wh2[ks], as_frag(u32x4{pk2(a[0], a[1]), pk2(a[2], a[3]), pk2(cc[0], cc[1]), pk2(cc[2], cc[3])}), zm);
  }
  float ps = (pp[0] + pp[1]) + (pp[2] + pp[3]);
  ps += dppf<0xB1>(ps);
  ps += dppf<0x4E>(ps);
  asm volatile("buffer_store_short %0, %1, %2, 0 offen" ::"v"(pk2(nd.pold + ps * NLN2, 0.f)), "v"(op), "s"(osrd) : "memory");
  float o[4];
#pragma unroll
  for (int i = 0; i < 4; ++i) { const float zz = nd.z[i] + zm[i] * NLN2; o[i] = zz * __builtin_amdgcn_rcpf(1.f + __builtin_amdgcn_exp2f(zz * NLOG2E)); }
  const u32x2 ov = {pk2(b2f(nd.hr[0]) + o[0], bhi(nd.hr[0]) + o[1]), pk2(b2f(nd.hr[1]) + o[2], bhi(nd.hr[1]) + o[3])};
  asm volatile("buffer_store_dwordx2 %0, %1, %2, 0 offen" ::"v"(ov), "v"(oh), "s"(osrd) : "memory");
}

__global__ __launch_bounds__(NW * 64) void egnn_fused(const u16* h_, const u16* pos_, const void* ea_, const void* src_, const void* dst_,
                                                      const u16* we_, const u16* wx_, const u16* wh_, char* ws_, u16* out_) {
  STAMP_DECL
  STAMP(0);
  u64x8 ka; u64x2 kb;
  asm volatile("s_load_dwordx16 %0, %2, 0x0\n\ts_load_dwordx4 %1, %2, 0x40" : "=&s"(ka), "=&s"(kb) : "s"(__builtin_amdgcn_kernarg_segment_ptr()) : "memory");
  u64 pc;
  asm volatile("s_getpc_b64 %0" : "=s"(pc));
  const int tid = threadIdx.x;
  int lane = tid & 63;
  const int w = __builtin_amdgcn_readfirstlane(tid >> 6);
  const int c = blockIdx.x;
  const int b = (c & 7) * 32 + (c >> 3);
  const int n0 = first_node(b), nn = first_node(b + 1) - n0;
  const u32 stg = L_STG + (u32)w * STGW, dsc = L_D + (u32)w * DSTR;
  if (tid < CODE_LINES) asm volatile("s_mov_b32 m0, %2\n\ts_nop 0\n\tglobal_load_lds_dword %0, %1" ::"v"((u32)(tid * 128)), "s"(pc), "s"(L_WH + 12288) : "memory");
  {
    constexpr u32 NZ = (L_NST - L_ZERO) / 16;
    if (tid < NZ + 64) lds_st<u32x4>(tid < NZ ? L_ZERO + tid * 16 : L_HIST + (tid - NZ) * 16, u32x4{0, 0, 0, 0});
    if (tid < NW * DSTR / 16) lds_st<u32x4>(L_D + tid * 16, u32x4{0, 0, 0, 0});
    if (w == 10) lds_st<u32>(L_NST + 1024 + lane * 4, L_S + (CAP + 64) * 8);
    if (w == 11) lds_st<u32>(L_OH + lane * 4, (((lane >> 3) & 3) == (lane & 3)) ? (((lane >> 2) & 1) ? 0x3F800000u : 0x3F80u) : 0u);
  }
  asm volatile("s_waitcnt lgkmcnt(0)" ::: "memory");
  __builtin_amdgcn_sched_barrier(0);
  asm volatile("" : "+s"(ka), "+s"(kb));
  const GAS u16* h = (const GAS u16*)ka[0]; const GAS u16* pos = (const GAS u16*)ka[1];
  const GAS char* ea4 = (const GAS char*)ka[2]; const GAS char* src4 = (const GAS char*)ka[3]; const GAS char* dst4 = (const GAS char*)ka[4];
  const GAS u16* we = (const GAS u16*)ka[5]; const GAS u16* wx = (const GAS u16*)ka[6]; const GAS u16* wh = (const GAS u16*)ka[7];
  GAS char* ws = (GAS char*)kb[0];
  const u32 epoch = *(const __attribute__((address_space(4))) u32*)(kb[0] + WS_EP);
  const u32 tag = epoch + 0x9E3779B1u, tag8 = (tag * 0x01010101u) & 0xff000000u;
  const u64 ftag = ((u64)tag << 32) | ~tag;

  const bool act1 = w * 256 + 128 + 2 * lane < CH;
  u32x2 d0, d1, s0, s1;
  u32x4 e0, e1;
  {
    const u32 q0 = (u32)(c * CH + (w < 10 ? w * 256 + 2 * lane : 0)), q1 = act1 ? q0 + 128 : q0;
    asm volatile("global_load_dwordx2 %0, %4, %6 nt\n\tglobal_load_dwordx2 %1, %5, %6 nt\n\tglobal_load_dwordx2 %2, %4, %7 nt\n\tglobal_load_dwordx2 %3, %5, %7 nt"
                 : "=&v"(d0), "=&v"(d1), "=&v"(s0), "=&v"(s1) : "v"(q0 * 4), "v"(q1 * 4), "s"(dst4), "s"(src4) : "memory");
    asm volatile("s_barrier" ::: "memory");
    asm volatile("global_load_dwordx4 %0, %2, %4 nt\n\tglobal_load_dwordx4 %1, %3, %4 nt" : "=&v"(e0), "=&v"(e1) : "v"(q0 * 8), "v"(q1 * 8), "s"(ea4) : "memory");
  }
  if (w >= 10) {
    const int m = 11 - w;
#pragma unroll
    for (int i = 0; i < 3; ++i) dma16(h, umin((u32)(n0 * 128 + (3 * m + i) * 1024 + lane * 16), (u32)(NN * 128 - 16)), L_HR + (u32)(3 * m + i) * 1024);
    asm volatile("s_mov_b32 m0, %2\n\ts_nop 0\n\tglobal_load_lds_dword %0, %1" ::"v"(m ? umin((u32)((c >> 3) * 1920 + (lane & 31) * 64), (u32)(NN * 6 - 4)) : (u32)((c >> 3) * 512 + (lane & 7) * 64)),
                 "s"(m ? pos : wh), "s"(L_SINK) : "memory");
#pragma unroll
    for (int i = 0; i < 8; ++i) dma16(we, (u32)((8 * m + i) * 1024 + lane * 16), L_WE + (u32)(8 * m + i) * 1024);
  }
  Win W;
  init_window(W, lane, stg);
  const int seg = w * SW + lane;
  const bool sv = lane < SW && seg < NBK;
  const int etile = w % 3, eT = w / 3;
  u32 taba = (u32)(((sv ? seg : c) * NBK + 2 * (b >> 2) + (b & 1) + 128 * ((b >> 1) & 1)) * 8), flea = (u32)((sv ? seg : c) * 1024 + (c & 7) * 128);
  u32 segbase = (u32)seg * CHS * 8, rep4 = (lane & 3) * 4, dummy = 1024u + lane * 4, bpf = umin((u32)((c >> 3) * 40000 + tid * 64), (u32)(NN * 128 - 64));
  u32 ha, hra, wa, pda, oh, op, mo, po, mska, mskp, fa, fp, whv;
  {
    const int nl = etile * 16 + (lane & 15), g = lane >> 4, pi = w * 16 + (lane >> 2);
    const int pn = pi < 120 ? (pi * 43) >> 7 : 63, pc3 = pi - pn * 3;
    ha = L_HR + nl * 128 + g * 16; hra = L_HR + nl * 128 + eT * 32 + g * 8;
    wa = L_WH + (u32)(eT * 4096 + (8 * g + ((lane >> 2) & 3)) * 32 + 8 * (lane & 3));
    whv = (u32)((lane >> 1) * 128 + eT * 32 + (lane & 1) * 16);
    pda = L_PD + (pi < 120 ? pn * 16 + pc3 * 4 : 0);
    oh = nl < nn ? (u32)((n0 + nl) * 128 + eT * 32 + g * 8) : 0x80000000u;
    op = pi < nn * 3 && (lane & 3) == 0 ? (u32)(NN * 128 + (n0 * 3 + pi) * 2) : 0x80000000u;
    mo = L_STG + (u32)((nl & 7) * MROW + 8 * g) * 4; po = L_STG + PART_P + (u32)((pn & 7) * 48 + pc3 * 16 + (lane & 3) * 4) * 4;
    mska = L_MSK + nl * 4; mskp = L_MSK + pn * 4;
    fa = stg + (u32)((lane >> 5) * MROW + (lane & 31)) * 4; fp = stg + PART_P + (u32)((lane >> 5) * 48 + ((lane & 31) >> 1)) * 4;
  }
  asm volatile("" : "+v"(taba), "+v"(flea), "+v"(segbase), "+v"(rep4), "+v"(dummy), "+v"(bpf), "+v"(ha), "+v"(hra), "+v"(wa), "+v"(pda), "+v"(oh), "+v"(op),
               "+v"(mo), "+v"(po), "+v"(mska), "+v"(mskp), "+v"(fa), "+v"(fp), "+v"(whv));
  const u32x4 psrd = make_srd(ka[1], 0, NN * 6), srd = make_srd(kb[0] + WS_BP, 128, NN), osrd = make_srd(kb[1], 0, NN * 134);
  int pslot = w + (c >> 3); pslot -= pslot >= 24 ? 24 : 0; pslot -= pslot >= 12 ? 12 : 0; pslot -= pslot >= 12 ? 12 : 0;
  const u32 padoff = ((u32)c * CHS + CH) * 8, bkey = (u32)b << 24;
  asm volatile("" ::"s"(psrd), "s"(srd), "s"(osrd), "s"(pslot), "s"(padoff), "s"(bkey));
  if (LIKELY(w < 10)) {
    STAMP(1);
    u32 bk[4], rk[4];
    WAITV(4); asm volatile("" : "+v"(d0), "+v"(d1));
    u32 d[4] = {d0[0], d0[1], d1[0], d1[1]};
#pragma unroll
    for (int k = 0; k < 4; ++k) bk[k] = __umul24(d[k], 214749u) >> 23;
#pragma unroll
    for (int k = 0; k < 4; ++k) rk[k] = lds_add(L_HIST + bk[k] * 4, k < 2 || act1 ? 1u : 0u);
    arrive(SY0);
#pragma unroll
    for (int k = 0; k < 4; ++k) d[k] = ((d[k] - (u32)first_node(bk[k])) << 18) | (bk[k] << 24) | (k < 2 || act1 ? 0u : 0xfc0000u);
    asm volatile("" : "+v"(d[0]), "+v"(d[1]), "+v"(d[2]), "+v"(d[3]));
    STAMP(2);
    await(SY0, 10);
    const u32x4 cn = lds_ld<u32x4>(L_HIST + lane * 16);
    const u32 tot = cn[0] + cn[1] + cn[2] + cn[3];
    const u32 ex = scan_add(tot) - tot;
    lds_st<u32x4>(L_OFFS + lane * 16, u32x4{ex, ex + cn[0], ex + cn[0] + cn[1], ex + cn[0] + cn[1] + cn[2]});
    asm volatile("" ::: "memory");
#pragma unroll
    for (int k = 0; k < 4; ++k) { rk[k] += lds_ld<u32>(L_OFFS + bk[k] * 4); if (k >= 2) rk[k] = umin(rk[k] + (act1 ? 0u : 65536u), CH + (k & 1)); rk[k] *= 8; }
    WAITV(2); asm volatile("" : "+v"(s0), "+v"(s1));
    const u32 s[4] = {s0[0], s0[1], s1[0], s1[1]};
#pragma unroll
    for (int k = 0; k < 4; ++k) lds_st<u32>(L_SIDX + (rk[k] >> 1), s[k] | d[k]);
    WAITV(1); asm volatile("" : "+v"(e0));
    lds_st<u32x2>(L_SEA + rk[0], u32x2{e0[0], e0[1]}); lds_st<u32x2>(L_SEA + rk[1], u32x2{e0[2], e0[3]});
    WAITV(0); asm volatile("" : "+v"(e1));
    lds_st<u32x2>(L_SEA + rk[2], u32x2{e1[0], e1[1]}); lds_st<u32x2>(L_SEA + rk[3], u32x2{e1[2], e1[3]});
    arrive(SY1A);
    STAMP(3);
    await(SY1A, 10);
    st16_dev(ws, WS_IDX + (u32)c * CHS * 4 + tid * 16, lds_ld<u32x4>(L_SIDX + tid * 16));
    u32x4 x = lds_ld<u32x4>(L_SEA + tid * 16), y = lds_ld<u32x4>(L_SEA + 10240 + tid * 16);
    u32x4 t0 = {(ex | (cn[0] << 12)) | tag8, tag, ((ex + cn[0]) | (cn[1] << 12)) | tag8, tag};
    u32x4 t1 = {((ex + cn[0] + cn[1]) | (cn[2] << 12)) | tag8, tag, ((ex + tot - cn[3]) | (cn[3] << 12)) | tag8, tag};
    u32 ta = WS_TAB + (u32)c * 2048 + lane * 16, eao = WS_EA + (u32)c * CHS * 8 + tid * 16;
    asm volatile("s_waitcnt vmcnt(0)" : "+v"(x), "+v"(y), "+v"(t0), "+v"(t1), "+v"(ta), "+v"(eao)::"memory");
    if (ticket(SY1B, 1u) == 9) {
      st16_dev(ws, ta, t0);
      st16_dev(ws + 1024, ta, t1);
      arrive(SY1B);
    }
    await(SY1B, 11);
    pos_pieces(psrd, lane * 16 + (u32)pslot * 5120, (u32)pslot * 5120);
    EDELAY;
    st16_dev(ws, eao, x);
    st16_dev(ws + 10240, eao, y);
    STAMP(4);
  } else {
    const int m = 11 - w;
    const GAS u16* pp = m ? pos + (n0 + (lane < nn ? lane : 0)) * 3 : we + 128 * 64 + lane;
    u32 pv[5];
#pragma unroll
    for (int i = 0; i < 5; ++i) pv[i] = pp[m ? (i < 3 ? i : 0) : 64 * i];
    const u32 wxv = wx[lane];
    STAMP(1);
    WAITV(0); asm volatile("" : "+v"(d0), "+v"(d1), "+v"(s0), "+v"(s1), "+v"(e0), "+v"(e1));
    arrive(SYH);
    await(SYH, 2);
    STAMP(2);
    const u32 ro16 = (u32)((lane & 15) * 128 + (lane >> 4) * 16);
    u32x4 hf[3][2];
#pragma unroll
    for (int t = 0; t < 3; ++t) { hf[t][0] = lds_ld<u32x4>(L_HR + t * 2048 + ro16); hf[t][1] = lds_ld<u32x4>(L_HR + t * 2048 + ro16 + 64); }
    const u32 a0 = L_WE + 8192 * m + (u32)((8 * (lane >> 4) + ((lane >> 2) & 3)) * 128 + 16 * (lane & 3));
    u32x4 v[2][3];
#pragma unroll
    for (int hv = 0; hv < 2; ++hv) {
      bf16x8 wfr[2][2];
#pragma unroll
      for (int T = 0; T < 2; ++T)
#pragma unroll
        for (int ks = 0; ks < 2; ++ks) { const u32 a = a0 + 4096 * ks + 64 * hv + 8 * T; wfr[T][ks] = as_frag(tr_read(a), tr_read(a + 512)); }
#pragma unroll
      for (int t = 0; t < 3; ++t)
#pragma unroll
        for (int T = 0; T < 2; ++T) {
          f32x4 acc = {0.f, 0.f, 0.f, 0.f};
          acc = MFMA16(wfr[T][0], as_frag(hf[t][0]), acc); acc = MFMA16(wfr[T][1], as_frag(hf[t][1]), acc);
          v[hv][t][2 * T] = pk2(acc[0] * NLOG2E, acc[1] * NLOG2E); v[hv][t][2 * T + 1] = pk2(acc[2] * NLOG2E, acc[3] * NLOG2E);
        }
    }
    if (m) {
#pragma unroll
      for (int t = 0; t < 3; ++t) {
        if (t < 2 || 32 + (lane & 15) < nn) { st16_dev(ws + WS_BP + t * 2048, (u32)n0 * 128 + ro16, v[0][t]); st16_dev(ws + WS_BP + t * 2048 + 64, (u32)n0 * 128 + ro16, v[1][t]); }
      }
      if (lane < 40) lds_st<u32x4>(L_PD + lane * 16, u32x4{pv[0] << 16, pv[1] << 16, pv[2] << 16, (L_OH - POFF + (lane & 7) * 16) << 16});
      lds_st<u32>(L_WX + lane * 4, wxv << 16);
    } else {
#pragma unroll
      for (int t = 0; t < 3; ++t) {
        lds_st<u32x4>(L_G + t * 2048 + ro16, v[0][t]); lds_st<u32x4>(L_G + t * 2048 + ro16 + 64, v[1][t]);
      }
#pragma unroll
      for (int r = 0; r < 8; ++r) lds_st<u16>(L_G + (48 + r) * 128 + lane * 2, r < 4 ? (u16)pk2(b2f(pv[1 + r]) * NLOG2E, 0.f) : (r == 5 ? (u16)pv[0] : (u16)0));
    }
    STAMP(3);
    await(SY1B, 11);
    pos_pieces(psrd, lane * 16 + (u32)pslot * 5120, (u32)pslot * 5120);
    STAMP(4);
  }
  asm volatile("s_waitcnt vmcnt(0)" ::: "memory");
  if (ticket(SY2, 1u) == NW - 1) st16_dev(ws + WS_FLE, (u32)c * 1024 + lane * 16, u32x4{~tag, tag, ~tag, tag});
  u64 g;
  if (TMODE) await(SY2, TMODE);
  STAMP(5);
  g = poll64(ws + WS_TAB, taba);

  if (UNLIKELY(!__all(!sv || ((u32)(g >> 32) == tag && ((u32)g ^ tag8) < 0x1000000u)))) {
#pragma unroll 1
    for (u32 spins = 0; spins < SPIN_CAP; ++spins) {
      g = poll64(ws + WS_TAB, taba);
      if (__all(!sv || ((u32)(g >> 32) == tag && ((u32)g ^ tag8) < 0x1000000u))) break;
      g = 0;
      __builtin_amdgcn_s_sleep(2);
    }
  }
  const u32 te = sv ? (u32)g & 0xffffffu : 0u;
  u32x2 ridx[PASSES];
  u32 ro[PASSES], Tw, nv;
  u64 on[PASSES];
  flatten<PASSES>(te, segbase, lane, padoff, dsc, 0u, ro, on, Tw, nv);
  fetch_index<PASSES>(ws, ro, ridx);
  u64 fle;
  asm volatile("global_load_dwordx2 %0, %1, %2 sc1" : "=&v"(fle) : "v"(flea), "s"(ws + WS_FLE) : "memory");
  const u32 rbase = ticket(L_FLG + 4, 2 * nv);
  const bool rok = rbase + 2 * nv <= (u32)RCAP;
  if (UNLIKELY(Tw > (u32)QFAST || !rok)) lds_st<u32>(L_FLG, 1u);
  const u32 eptr = L_EA - POFF + rbase * 8 + lane * 16;
  STAMP(6);
  WAITV(1);
#pragma unroll
  for (int k = 0; k < PASSES; ++k) asm volatile("" : "+v"(ridx[k]));
  STAMP(7);
  u32 rrank[4], ca[4], rrank2[2], ca2[2];
  u32x2 sent[4], rdv[4], sent2[2], rdv2[2];
  rank_records<2>(rep4, dummy, bkey, ridx, rrank, ca);
  if (UNLIKELY(nv > 128)) rank_records<1>(rep4, dummy, bkey, ridx + 2, rrank2, ca2);
  arrive(SY5);
  WAITV(0); asm volatile("" : "+v"(fle));
  const bool early = __all(!sv || fle == ftag);
  if (early && rok) fetch_attrs<PASSES>(ws, ro, on, rbase);
  await(SY2, NW);
  geometry<2>(eptr, ridx, ca, sent, rdv);
  if (UNLIKELY(nv > 128)) geometry<1>(eptr + 2048, ridx + 2, ca2, sent2, rdv2);
  asm volatile("global_load_dwordx2 %0, %1, %2 sc1" : "=&v"(fle) : "v"(flea), "s"(ws + WS_FLE) : "memory");
  STAMP(8);
  await(SY5, NW);
  STAMP(9);
  asm volatile("s_mov_b32 m0, %2\n\ts_nop 0\n\tglobal_load_lds_dword %0, %1 sc1" ::"v"(BPF ? bpf : 0u), "s"(ws + WS_BP), "s"(L_SINK) : "memory");
  const u32x4 cnt = lds_ld<u32x4>(L_CNT + lane * 16);
  const u32 flg = lds_ld<u32>(L_FLG);
  u32 nstart, total;
  scan_nodes(cnt, lane, nstart, total);
  bool rare = __builtin_amdgcn_readfirstlane(flg) != 0 || total > (u32)CAP;
  u32 sbeg = 0, send = 0;
  int fn = 0, ln = 0;
  if (LIKELY(!rare)) {
    scatter_records<2>(rrank, ca, sent, rdv);
    if (UNLIKELY(nv > 128)) scatter_records<1>(rrank2, ca2, sent2, rdv2);
    finish_lists(lane, w, nstart, total, sbeg, send);
    fn = __builtin_popcountll(__ballot(nstart <= sbeg && lane < nn)) - 1;
    ln = __builtin_popcountll(__ballot(nstart < send && lane < nn)) - 1;
    if (UNLIKELY(ln - fn >= 8 || sbeg >= send)) lds_st<u32>(L_FLG + 8, 1u);
    if (lane >= fn && lane <= ln) __hip_atomic_fetch_or((LAS u32*)(size_t)(L_MSK + lane * 4), 1u << w, __ATOMIC_RELAXED, __HIP_MEMORY_SCOPE_WORKGROUP);
    setup_window(W, lane, nn, fn, sbeg);
  }
  STAMP(10);
  WAITV(1); asm volatile("" : "+v"(fle));
  if (!early) {
#pragma unroll 1
    for (u32 spins = 0; spins < SPIN_CAP; ++spins) {
      if (__all(!sv || fle == ftag)) break;
      fle = poll64(ws + WS_FLE, flea);
    }
    if (!rare) fetch_attrs<PASSES>(ws, ro, on, rbase);
  }
  WAITV(0);
  wg_barrier();
  STAMP(11); CYC(16);
  if (LIKELY(!rare)) {
    first_tile(W, stg, srd);
    rare = __builtin_amdgcn_readfirstlane(lds_ld<u32>(L_FLG + 8)) != 0;
  }
  if (LIKELY(!rare)) {
    load_wh(wh, whv, L_WH + eT * 4096);
    u32 mk = lds_ld<u32>(mska), pk = lds_ld<u32>(mskp), ma[2], pa[2];
    float wx0 = lds_ld<float>(L_WX + (lane & 31) * 4), wx1 = lds_ld<float>(L_WX + 128 + (lane & 31) * 4);
#pragma unroll
    for (int u = 0; u < 2; ++u) {
      ma[u] = mk ? (u32)__builtin_ctz(mk) * STGW + mo : L_ZERO;
      pa[u] = pk ? (u32)__builtin_ctz(pk) * STGW + po : L_ZERO;
      mk &= mk - 1; pk &= pk - 1;
    }
    const bool more = __ballot(mk | pk) != 0;
    const u32 touched = ((2u << (ln - fn)) - 1u) * 0x101u >> (8 - (fn & 7));
    asm volatile("" : "+v"(ma[0]), "+v"(ma[1]), "+v"(pa[0]), "+v"(pa[1]), "+v"(wx0), "+v"(wx1));
    run_tiles(W, stg, srd, sbeg, send);
    STAMP(12); CYC(17);
    Node nd;
#pragma unroll
    for (int g4 = 0; g4 < 4; ++g4) {
      if ((touched >> (2 * g4)) & 3u) {
        lds_st<float>(fa + g4 * 2 * MROW * 4, W.acc0[4 * g4]);
        lds_st<float>(fa + g4 * 2 * MROW * 4 + 128, W.acc1[4 * g4]);
#pragma unroll
        for (int cc = 0; cc < 3; ++cc) {
          const float v = W.acc0[4 * g4 + 1 + cc] * wx0 + W.acc1[4 * g4 + 1 + cc] * wx1;
          lds_st<float>(fp + g4 * 384 + 64 * cc, v + dppf<0xB1>(v));
        }
      }
    }
    node_operands(nd, ha, hra, wa, pda);
    STAMP(13);
    wg_barrier();
    STAMP(14);
    f32x4 mm[4], nm[4], pp, np;
#pragma unroll
    for (int i = 0; i < 4; ++i) { mm[i] = lds_ld<f32x4>(ma[0] + 128 * (i >> 1) + 16 * (i & 1)); nm[i] = lds_ld<f32x4>(ma[1] + 128 * (i >> 1) + 16 * (i & 1)); }
    pp = lds_ld<f32x4>(pa[0]); np = lds_ld<f32x4>(pa[1]);
    __builtin_amdgcn_sched_barrier(0);
#pragma unroll
    for (int i = 0; i < 4; ++i) mm[i] += nm[i];
    pp += np;
    if (UNLIKELY(more)) {
      u32 m2 = lds_ld<u32>(mska), p2 = lds_ld<u32>(mskp);
      m2 &= m2 - 1; m2 &= m2 - 1; p2 &= p2 - 1; p2 &= p2 - 1;
#pragma unroll 1
      while (__ballot(m2 | p2)) {
        const u32 a = m2 ? (u32)__builtin_ctz(m2) * STGW + mo : L_ZERO, a2 = p2 ? (u32)__builtin_ctz(p2) * STGW + po : L_ZERO;
        m2 &= m2 - 1; p2 &= p2 - 1;
#pragma unroll
        for (int i = 0; i < 4; ++i) mm[i] += lds_ld<f32x4>(a + 128 * (i >> 1) + 16 * (i & 1));
        pp += lds_ld<f32x4>(a2);
      }
    }
    node_update(nd, mm, pp, osrd, oh, op);
  } else {
    for (u32 i = w * 64 + lane; i < ACCS / 16; i += NW * 64) st16_dev(ws, WS_ACC + (u32)c * ACCS + i * 16, u32x4{0, 0, 0, 0});
    if (lane == 0) __hip_atomic_fetch_max((LAS u32*)(size_t)(L_FLG + 12), Tw, __ATOMIC_RELAXED, __HIP_MEMORY_SCOPE_WORKGROUP);
    GAS float* ga = (GAS float*)(ws + WS_ACC + (u32)c * ACCS);
#pragma unroll 1
    for (u32 vbase = 0;; vbase += 64) {
      asm volatile("s_waitcnt vmcnt(0)" ::: "memory");
      wg_barrier();
      asm volatile("" : "+v"(lane));
      if (vbase >= (u32)__builtin_amdgcn_readfirstlane(lds_ld<u32>(L_FLG + 12))) break;
      if (w == 0) { lds_st<u32x4>(L_CNT + lane * 16, u32x4{0, 0, 0, 0}); lds_st<u32>(L_CNT + 1024 + lane * 4, 0u); lds_st<u32>(L_FLG + 4, 0u); }
      lds_st<u32>(dsc + lane * 4, 0u);
      pos_pieces(psrd, lane * 16 + (u32)w * 5120, (u32)w * 5120);
      wg_barrier();
      u32x2 ridx1[1], sent1[2], rdv1[2];
      u64 on1[1];
      u32 ro1[1], rrank1[2], ca1[2], Tw1, nv1, sb, se, nst, tot;
      flatten<1>(te, (u32)(w * SW + lane) * CHS * 8, lane, padoff, dsc, vbase, ro1, on1, Tw1, nv1);
      const u32 rbase1 = ticket(L_FLG + 4, 2 * nv1);
      fetch_index<1>(ws, ro1, ridx1);
      fetch_attrs<1>(ws, ro1, on1, rbase1);
      WAITV(0); asm volatile("" : "+v"(ridx1[0]));
      rank_records<1>((lane & 3) * 4, 1024u + lane * 4, bkey, ridx1, rrank1, ca1);
      wg_barrier();
      geometry<1>(L_EA - POFF + rbase1 * 8 + lane * 16, ridx1, ca1, sent1, rdv1);
      scan_nodes(lds_ld<u32x4>(L_CNT + lane * 16), lane, nst, tot);
      scatter_records<1>(rrank1, ca1, sent1, rdv1);
      finish_lists(lane, w, nst, tot, sb, se);
      wg_barrier();
#pragma unroll 1
      while (sb < se) {
        asm volatile("" : "+v"(lane));
        const int base = __builtin_popcountll(__ballot(nst <= sb && lane < nn)) - 1;
        const u32 wend = umin(__builtin_amdgcn_readlane(nst, base + 8 < nn ? base + 8 : nn), se);
        Win V;
        init_window(V, lane, stg);
        setup_window(V, lane, nn, base, sb);
        first_tile(V, stg, srd);
        run_tiles(V, stg, srd, sb, wend);
        asm volatile("" : "+v"(lane));
        const float wx0 = lds_ld<float>(L_WX + (lane & 31) * 4), wx1 = lds_ld<float>(L_WX + 128 + (lane & 31) * 4);
        const int lastn = __builtin_popcountll(__ballot(nst < wend && lane < nn)) - 1;
#pragma unroll
        for (int g4 = 0; g4 < 4; ++g4) {
          float pv[3];
#pragma unroll
          for (int cc = 0; cc < 3; ++cc) {
            const float v = V.acc0[4 * g4 + 1 + cc] * wx0 + V.acc1[4 * g4 + 1 + cc] * wx1;
            pv[cc] = v + dppf<0xB1>(v);
          }
          const int nd = base + ((2 * g4 + (lane >> 5) - base) & 7);
          if (nd <= lastn) {
            __hip_atomic_fetch_add(ga + nd * 112 + (lane & 31), V.acc0[4 * g4], RLX_AGENT);
            __hip_atomic_fetch_add(ga + nd * 112 + 32 + (lane & 31), V.acc1[4 * g4], RLX_AGENT);
            if (!(lane & 1)) {
#pragma unroll
              for (int cc = 0; cc < 3; ++cc) __hip_atomic_fetch_add(ga + nd * 112 + 64 + 16 * cc + ((lane & 31) >> 1), pv[cc], RLX_AGENT);
            }
          }
        }
        sb = wend;
      }
    }
    load_wh(wh, whv, L_WH + eT * 4096);
    asm volatile("s_waitcnt vmcnt(0)" ::: "memory");
    Node nd;
    node_operands(nd, ha, hra, wa, pda);
    const int nl = etile * 16 + (lane & 15), pi = w * 16 + (lane >> 2);
    const int nq = (nl < 40 ? nl : 0) * 112 + 8 * (lane >> 4), pn = pi < 120 ? pi / 3 : 0, pq = pn * 112 + 64 + (pi < 120 ? pi - pn * 3 : 0) * 16 + (lane & 3) * 4;
    f32x4 mm[4], pp;
#pragma unroll
    for (int i = 0; i < 4; ++i) {
#pragma unroll
      for (int k = 0; k < 4; ++k) mm[k][i] = __hip_atomic_load(ga + nq + 32 * (k >> 1) + 4 * (k & 1) + i, RLX_AGENT);
      pp[i] = __hip_atomic_load(ga + pq + i, RLX_AGENT);
    }
    node_update(nd, mm, pp, osrd, oh, op);
  }
  if (c == 0 && tid == 0) __hip_atomic_store((GAS u32*)(ws + WS_EP), tag, RLX_AGENT);
}

extern "C" void kernel_launch(void* const* d_in, const int* in_sizes, int n_in, void* d_out, int out_size, void* d_ws, size_t ws_size,
                              hipStream_t stream) {
  (void)hipFuncSetAttribute((const void*)egnn_fused, hipFuncAttributeMaxDynamicSharedMemorySize, (int)L_END);
  egnn_fused<<<NBK, NW * 64, L_END, stream>>>((const u16*)d_in[0], (const u16*)d_in[1], d_in[2], d_in[3],
                                              d_in[4], (const u16*)d_in[5], (const u16*)d_in[6], (const u16*)d_in[7],
                                              (char*)d_ws + WS_BASE, (u16*)d_out);
}
```

```cpp
#include <hip/hip_runtime.h>
#include <hip/hip_bf16.h>
#include <stdint.h>

typedef unsigned short u16;
typedef unsigned int u32;
typedef unsigned long long u64;
typedef __attribute__((ext_vector_type(16))) float f32x16;
typedef __attribute__((ext_vector_type(4))) float f32x4;
typedef __attribute__((ext_vector_type(2))) float f32x2;
typedef __attribute__((ext_vector_type(8))) __bf16 bf16x8;
typedef __attribute__((ext_vector_type(2))) __bf16 bf16x2;
typedef __attribute__((ext_vector_type(4))) short s16x4;
typedef __attribute__((ext_vector_type(4))) u32 u32x4;
typedef __attribute__((ext_vector_type(2))) u32 u32x2;
typedef __attribute__((ext_vector_type(8))) u64 u64x8;
typedef __attribute__((ext_vector_type(2))) u64 u64x2;
#define LAS __attribute__((address_space(3)))
#define GAS __attribute__((address_space(1)))
#define DI __device__ __forceinline__
#define RLX_AGENT __ATOMIC_RELAXED, __HIP_MEMORY_SCOPE_AGENT

constexpr int NN = 10000, NBK = 256, CH = 2500, CHS = 2560;
constexpr float NLOG2E = -1.4426950408889634f, NLN2 = -0.6931471805599453f;

constexpr u32 WS_IDX = 0;
constexpr u32 WS_EA  = WS_IDX + NBK * CHS * 4;
constexpr u32 WS_TAB = WS_EA + NBK * CHS * 8;
constexpr u32 WS_FLE = WS_TAB + NBK * NBK * 8;
constexpr u32 WS_BP  = WS_FLE + NBK * 1024;
constexpr u32 WS_EP  = WS_BP + NN * 128;
constexpr u32 WS_ACC = WS_EP + 256;
constexpr u32 ACCS = 40 * 112 * 4;
constexpr u32 WS_END = WS_ACC + NBK * ACCS;

DI float b2f(u32 x) { return __uint_as_float(x << 16); }
DI float bhi(u32 x) { return __uint_as_float(x & 0xffff0000u); }
DI u32 pk2(float a, float b) { f32x2 v = {a, b}; return __builtin_bit_cast(u32, __builtin_convertvector(v, bf16x2)); }
DI int first_node(int b) { return (b * 625 + 15) >> 4; }

template <int CTRL, int RM> DI u32 dpp0(u32 x) { return (u32)__builtin_amdgcn_update_dpp(0, (int)x, CTRL, RM, 0xf, false); }
DI u32 scan_add(u32 x) {
  x += dpp0<0x111, 0xf>(x); x += dpp0<0x112, 0xf>(x); x += dpp0<0x114, 0xf>(x); x += dpp0<0x118, 0xf>(x);
  x += dpp0<0x142, 0xa>(x); x += dpp0<0x143, 0xc>(x); return x;
}
DI u32 umax(u32 a, u32 b) { return a > b ? a : b; }
DI u32 umin(u32 a, u32 b) { return a < b ? a : b; }
DI u32 scan_max(u32 x) {
  x = umax(x, dpp0<0x111, 0xf>(x)); x = umax(x, dpp0<0x112, 0xf>(x)); x = umax(x, dpp0<0x114, 0xf>(x)); x = umax(x, dpp0<0x118, 0xf>(x));
  x = umax(x, dpp0<0x142, 0xa>(x)); x = umax(x, dpp0<0x143, 0xc>(x)); return x;
}
template <int CTRL> DI float dppf(float v) { return __uint_as_float((u32)__builtin_amdgcn_update_dpp(0, (int)__float_as_uint(v), CTRL, 0xf, 0xf, false)); }
DI u32x2 tr_read(u32 addr) { return __builtin_bit_cast(u32x2, __builtin_amdgcn_ds_read_tr16_b64_v4i16((LAS s16x4*)(size_t)addr)); }
template <typename T> DI T lds_ld(u32 addr) { return *(LAS T*)(size_t)addr; }
template <typename T> DI void lds_st(u32 addr, T v) { *(LAS T*)(size_t)addr = v; }
DI u32 lds_add(u32 addr, u32 v) { return __hip_atomic_fetch_add((LAS u32*)(size_t)addr, v, __ATOMIC_RELAXED, __HIP_MEMORY_SCOPE_WORKGROUP); }
DI void dma16(const GAS void* base, u32 voff, u32 ldsaddr) {
  asm volatile("s_mov_b32 m0, %2\n\ts_nop 0\n\tglobal_load_lds_dwordx4 %0, %1" ::"v"(voff), "s"(base), "s"(ldsaddr) : "memory");
}
DI void st16_dev(const GAS void* base, u32 voff, u32x4 v) {
  asm volatile("global_store_dwordx4 %0, %1, %2 sc1\n\ts_nop 1" ::"v"(voff), "v"(v), "s"(base) : "memory");
}
#define WAITV(n) WAITV_(n)
#define WAITV_(n) do { asm volatile("s_waitcnt vmcnt(" #n ")" ::: "memory"); __builtin_amdgcn_sched_barrier(0); } while (0)
DI void wg_barrier() { asm volatile("s_waitcnt lgkmcnt(0)\n\ts_barrier" ::: "memory"); }
DI bf16x8 as_frag(u32x4 v) { return __builtin_bit_cast(bf16x8, v); }
DI bf16x8 as_frag(u32x2 a, u32x2 b) { return __builtin_bit_cast(bf16x8, u32x4{a[0], a[1], b[0], b[1]}); }

#define CODE_LINES 164
constexpr int NW = 12, PASSES = 3, CAP = 2880, RCAP = 3200, QFAST = PASSES * 64;
constexpr int STGS = 1088, STGW = 4 * STGS, MROW = 36;
constexpr u32 POFF = 244;
constexpr u32 L_WX = 0;
constexpr u32 L_EA = 256;
constexpr u32 L_OH = L_EA + RCAP * 8;
constexpr u32 L_ZERO = L_OH + 256;
constexpr u32 L_SINK = L_ZERO + 1024;
constexpr u32 L_CNT = L_ZERO + 3328;
constexpr u32 L_MSK = L_CNT + 1280;
constexpr u32 L_FLG = L_MSK + 256;
constexpr u32 L_NST = L_FLG + 64;
constexpr u32 L_PD = L_NST + 1280;
constexpr u32 L_G = 33024;
constexpr u32 L_HR = L_G + 56 * 128;
constexpr u32 L_STG = L_HR + 6144;
constexpr u32 L_WH = L_STG + NW * STGW;
constexpr u32 L_S = L_WH + 16384;
constexpr u32 L_DV = L_S + (CAP + 144) * 8;
constexpr u32 L_END = L_DV + (CAP + 208) * 8;
constexpr u32 PART_P = 8 * MROW * 4;
constexpr u32 L_SEA = L_EA, L_HIST = L_SEA + CHS * 8, L_OFFS = L_HIST + 1024;
constexpr u32 L_POS = L_STG, L_SIDX = L_S, L_WE = L_SIDX + CHS * 4, L_D = L_WE + 16384, DSTR = QFAST * 4;
constexpr u32 SY0 = L_FLG + 16, SY1A = L_FLG + 20, SY1B = L_FLG + 24, SY2 = L_FLG + 28, SYH = L_FLG + 32, SY5 = L_FLG + 36, SY6 = L_FLG + 40, SY7 = L_FLG + 44;
static_assert(L_ZERO % 256 == 0 && L_STG % 256 == 0 && L_END <= 163840 && L_PD + 640 <= L_G && L_ZERO + 16 < 0x7F80, "lds layout");
static_assert(PART_P + 1616 + 1536 <= STGW, "partials");
static_assert(64 * NW * 2 <= CAP && CAP % 32 == 0 && L_OFFS + 1024 <= L_OH && L_D + NW * DSTR <= L_END && L_POS + 61440 <= L_S, "aliases");
constexpr u32 SPIN_CAP = 1u << 22;
#define MFMA16(a, b, c) __builtin_amdgcn_mfma_f32_16x16x32_bf16(a, b, c, 0, 0, 0)
#define BALLOT(x) __builtin_amdgcn_ballot_w64(x)
#define LIKELY(x) __builtin_expect(!!(x), 1)
#define UNLIKELY(x) __builtin_expect(!!(x), 0)

DI void arrive(u32 addr) {
  asm volatile("s_mov_b64 exec, 1\n\tds_add_u32 %0, %1\n\ts_mov_b64 exec, -1" ::"v"(addr), "v"(1u) : "memory");
}
DI u32 ticket(u32 addr, u32 v) {
  u32 r, t;
  asm volatile("s_mov_b64 exec, 1\n\tds_add_rtn_u32 %1, %2, %3\n\ts_mov_b64 exec, -1\n\ts_waitcnt lgkmcnt(0)\n\tv_readfirstlane_b32 %0, %1\n\ts_nop 3"
               : "=s"(r), "=&v"(t) : "v"(addr), "v"(v) : "memory");
  return r;
}
DI void await(u32 addr, u32 n) {
  while ((u32)__builtin_amdgcn_readfirstlane(__hip_atomic_load((LAS u32*)(size_t)addr, __ATOMIC_RELAXED, __HIP_MEMORY_SCOPE_WORKGROUP)) < n) {}
  asm volatile("" ::: "memory");
}
DI u32x4 make_srd(u64 base, u32 stride, u32 n) { return u32x4{(u32)base, (u32)(base >> 32) | (stride << 16), n, 0x00020000u}; }
DI void pos_pieces(u32x4 srd, u32 voff, u32 first) {
  asm volatile("s_mov_b32 m0, %2\n\ts_nop 0\n\tbuffer_load_dwordx4 %0, %1, 0 offen lds\n\tbuffer_load_dwordx4 %0, %1, 0 offen offset:1024 lds\n\t"
               "buffer_load_dwordx4 %0, %1, 0 offen offset:2048 lds\n\tbuffer_load_dwordx4 %0, %1, 0 offen offset:3072 lds\n\t"
               "s_add_u32 m0, %2, 1024\n\ts_nop 0\n\tbuffer_load_dwordx4 %3, %1, 0 offen offset:3072 lds"
               ::"v"(voff), "s"(srd), "s"(L_POS + first), "v"(voff + 1024) : "memory", "scc");
}
DI u64 poll64(const GAS char* base, u32 voff) {
  u64 g;
  asm volatile("global_load_dwordx2 %0, %1, %2 sc1\n\ts_waitcnt vmcnt(0)" : "=&v"(g) : "v"(voff), "s"(base) : "memory");
  return g;
}

template <int P>
DI void flatten(u32 te, u32 segbase, int lane, u32 padoff, u32 dsc, u32 vbase, u32 (&ro)[P], u64 (&on)[P], u32& Tw, u32& nv) {
  const u32 scnt = ((te >> 12) + 1) >> 1;
  const u32 sincl = scan_add(scnt);
  const u32 sexcl = sincl - scnt;
  Tw = __builtin_amdgcn_readlane(sincl, 63);
  const u32 delta = segbase + (te & 0xfffu) * 8 - 16 * sexcl;
  if (scnt > 0 && sexcl < vbase + 64 * P) {
    const u32 pp = sexcl > vbase ? sexcl - vbase : 0u;
    __hip_atomic_fetch_max((LAS u32*)(size_t)(dsc + pp * 4), delta, __ATOMIC_RELAXED, __HIP_MEMORY_SCOPE_WAVEFRONT);
  }
  nv = Tw > vbase ? umin(Tw - vbase, 64 * P) : 0u;
  asm volatile("" ::: "memory");
  u32 carry = 0;
#pragma unroll
  for (int k = 0; k < P; ++k) ro[k] = lds_ld<u32>(dsc + (64 * k + lane) * 4);
#pragma unroll
  for (int k = 0; k < P; ++k) {
    const u32 dl = umax(scan_max(ro[k]), carry);
    carry = __builtin_amdgcn_readlane(dl, 63);
    const bool act = 64 * k + lane < nv;
    on[k] = BALLOT(act);
    ro[k] = act ? 16 * (vbase + 64 * k + lane) + dl : padoff;
  }
}
template <int P>
DI void fetch_index(const GAS char* ws, const u32 (&ro)[P], u32x2 (&ridx)[P]) {
#pragma unroll
  for (int k = 0; k < P; ++k) asm volatile("global_load_dwordx2 %0, %1, %2 sc1" : "=&v"(ridx[k]) : "v"(ro[k] >> 1), "s"(ws + WS_IDX) : "memory");
}
template <int P>
DI void fetch_attrs(const GAS char* ws, const u32 (&ro)[P], const u64 (&on)[P], u32 rbase) {
#pragma unroll
  for (int k = 0; k < P; ++k)
    asm volatile("s_mov_b32 m0, %2\n\ts_mov_b64 exec, %3\n\ts_cbranch_execz 1f\n\tglobal_load_lds_dwordx4 %0, %1 sc1\n1:\n\ts_mov_b64 exec, -1"
                 ::"v"(ro[k]), "s"(ws + WS_EA), "s"(L_EA + (rbase + 128 * k) * 8), "s"(on[k]) : "memory");
}
template <int K>
DI void rank_records(u32 rep4, u32 dummy, u32 bkey, const u32x2* ridx, u32 (&rrank)[2 * K], u32 (&ca)[2 * K]) {
#pragma unroll
  for (int i = 0; i < 2 * K; ++i) {
    const u32 x = ridx[i >> 1][i & 1];
    ca[i] = (x ^ bkey) < (40u << 18) ? ((x >> 14) & 0x3f0u) | rep4 : dummy;
    rrank[i] = lds_add(L_CNT + ca[i], 1u);
  }
}
template <int K>
DI void load_geometry(const u32x2* ridx, const u32 (&ca)[2 * K], u32 (&a6)[2 * K], u32x2 (&ps)[2 * K], u32x4 (&pd)[2 * K]) {
#pragma unroll
  for (int i = 0; i < 2 * K; ++i) {
    a6[i] = (ridx[i >> 1][i & 1] & 0xffffu) * 6 + L_POS;
    const u32 pa = a6[i] & ~3u;
    ps[i] = u32x2{lds_ld<u32>(pa), lds_ld<u32>(pa + 4)};
    pd[i] = lds_ld<u32x4>(L_PD + (ca[i] & 0x3f0u));
  }
}
template <int K>
DI void geometry(u32 eptr, const u32x2* ridx, const u32 (&ca)[2 * K], const u32 (&a6)[2 * K], const u32x2 (&ps)[2 * K], const u32x4 (&pd)[2 * K],
                 u32 (&rrank)[2 * K], u32 (&rdvo)[2 * K], u32x2 (&sent)[2 * K], u32x2 (&rdv)[2 * K]) {
#pragma unroll
  for (int i = 0; i < 2 * K; ++i) {
    const u32 x = ridx[i >> 1][i & 1];
    const u32 sxy = __builtin_amdgcn_alignbyte(ps[i][1], ps[i][0], a6[i]), sz = __builtin_amdgcn_alignbyte(ps[i][1], ps[i][1], a6[i]);
    const float dx = __uint_as_float(pd[i][0]) - b2f(sxy);
    const f32x2 dyz = f32x2{__uint_as_float(pd[i][2]), __uint_as_float(pd[i][3])} - f32x2{bhi(sxy), b2f(sz)};
    const float d2 = __builtin_fmaf(dyz[1], dyz[1], __builtin_fmaf(dyz[0], dyz[0], dx * dx)) * NLOG2E;
    sent[i] = u32x2{(x & 0xffffu) | (pk2(d2, d2) & 0xffff0000u), eptr + (u32)((i >> 1) * 1024 + (i & 1) * 8) + pd[i][1]};
    rdv[i] = u32x2{pk2(1.0f, dx), pk2(dyz[0], dyz[1])};
    rdvo[i] = (rrank[i] + ((ca[i] >> 4) & 63u)) * 8;
    rrank[i] *= 8;
    asm volatile("" : "+v"(sent[i]), "+v"(rdv[i]), "+v"(rdvo[i]), "+v"(rrank[i]));
  }
}
template <int K>
DI void scatter_records(const u32 (&rrank)[2 * K], u32 (&rdvo)[2 * K], const u32 (&ca)[2 * K], const u32x2 (&sent)[2 * K]) {
  u32 st[2 * K];
#pragma unroll
  for (int i = 0; i < 2 * K; ++i) st[i] = lds_ld<u32>(L_NST + ca[i]);
#pragma unroll
  for (int i = 0; i < 2 * K; ++i) { lds_st<u32x2>(st[i] + rrank[i], sent[i]); rdvo[i] += st[i]; }
}
template <int K>
DI void scatter_diffs(const u32 (&rdvo)[2 * K], const u32x2 (&rdv)[2 * K]) {
#pragma unroll
  for (int i = 0; i < 2 * K; ++i) lds_st<u32x2>(rdvo[i] + (L_DV - L_S + 8), rdv[i]);
}
DI void scan_nodes(u32x4 cn, int lane, u32& nstart, u32& total) {
  const u32 tot = cn[0] + cn[1] + cn[2] + cn[3];
  const u32 incl = scan_add(tot);
  nstart = incl - tot;
  total = __builtin_amdgcn_readlane(incl, 63);
  const u32 a = L_S + nstart * 8;
  lds_st<u32x4>(L_NST + lane * 16, u32x4{a, a + cn[0] * 8, a + (cn[0] + cn[1]) * 8, a + (tot - cn[3]) * 8});
  asm volatile("" ::: "memory");
}
DI void finish_lists(int lane, int w, u32 nstart, u32 total, u32& sbeg, u32& send) {
  if (w == NW - 1) {
    lds_st<u32x2>(L_DV + (nstart + lane) * 8, u32x2{0, 0});
    lds_st<u32x2>(L_S + (total + lane) * 8, u32x2{0u, (L_ZERO - POFF) * 0x10001u});
  }
  const int ntiles = (int)(total + 31) >> 5;
  const int tq = ntiles / NW, tr = ntiles - tq * NW;
  const int tb = tq * w + (w < tr ? w : tr), tend = tb + tq + (w < tr ? 1 : 0);
  sbeg = (u32)tb * 32;
  send = umin((u32)tend * 32, total);
}

#define TILE_FIRST \
  "ds_read_u16 v92, %[s4a] offset:256\n\t" \
  "ds_read_u16 v94, %[s4a] offset:264\n\t" \
  "ds_read_u16 v96, %[s4a] offset:272\n\t" \
  "ds_read_u16 v98, %[s4a] offset:280\n\t" \
  "v_mov_b32 v93, %[lp]\n\t" \
  "v_mov_b32 v95, %[lp]\n\t" \
  "v_mov_b32 v97, %[lp]\n\t" \
  "v_mov_b32 v99, %[lp]\n\t" \
  "s_waitcnt lgkmcnt(0)\n\t" \
  "s_mov_b32 m0, %[stg]\n\t" \
  "s_nop 0\n\t" \
  "buffer_load_dwordx4 v[92:93], %[srd], 0 idxen offen sc1 lds\n\t" \
  "s_add_u32 m0, %[stg], 1088\n\t" \
  "s_nop 0\n\t" \
  "buffer_load_dwordx4 v[94:95], %[srd], 0 idxen offen sc1 lds\n\t" \
  "s_add_u32 m0, %[stg], 2176\n\t" \
  "s_nop 0\n\t" \
  "buffer_load_dwordx4 v[96:97], %[srd], 0 idxen offen sc1 lds\n\t" \
  "s_add_u32 m0, %[stg], 3264\n\t" \
  "s_nop 0\n\t" \
  "buffer_load_dwordx4 v[98:99], %[srd], 0 idxen offen sc1 lds\n\t"
#define TILE_FIRST_CLOBBERS "v92", "v93", "v94", "v95", "v96", "v97", "v98", "v99"
#define TILE_LOOP_QUAD \
  "ds_read_u16 v100, %[s1a] offset:256\n\t" \
  "v_mov_b32 v93, %[lp]\n\t" \
  "v_mov_b32 v95, %[lp]\n\t" \
  "v_mov_b32 v97, %[lp]\n\t" \
  "v_mov_b32 v99, %[lp]\n\t" \
  "v_subrev_u32 v101, 64, %[lo]\n\t" \
  "v_subrev_u32 v102, 64, %[hi]\n\t" \
  "v_subrev_u32 v103, 128, %[lo]\n\t" \
  "v_subrev_u32 v104, 128, %[hi]\n\t" \
  "v_subrev_u32 v105, 192, %[lo]\n\t" \
  "v_subrev_u32 v106, 192, %[hi]\n\t" \
  "v_mov_b32 v107, 1.0\n\t" \
  "v_mov_b32 v108, 1.0\n\t" \
  "v_mov_b32 v109, 1.0\n\t" \
  "v_mov_b32 v110, 1.0\n\t" \
  "v_mov_b32 v111, 1.0\n\t" \
  "v_mov_b32 v120, 1.0\n\t" \
  "v_mov_b32 v121, 1.0\n\t" \
  "v_mov_b32 v122, 1.0\n\t" \
  "v_mov_b32 v123, 1.0\n\t" \
  "s_waitcnt lgkmcnt(0)\n\t" \
  "v_bfi_b32 v64, %[mh], v100, %[s1a]\n\t" \
  "ds_read_b64 v[88:89], v100 offset:244\n\t" \
  "ds_read_b64 v[90:91], v64 offset:252\n\t" \
  "ds_read_u16 v100, %[s1a] offset:512\n\t" \
  "ds_read_u16 v92, %[s4a] offset:512\n\t" \
  "ds_read_u16 v94, %[s4a] offset:520\n\t" \
  "ds_read_u16 v96, %[s4a] offset:528\n\t" \
  "ds_read_u16 v98, %[s4a] offset:536\n\t" \
  "v_add_u32 %[s1a], 0x100, %[s1a]\n\t" \
  "v_add_u32 %[s4a], 0x100, %[s4a]\n\t" \
  "1:\n\t" \
  "s_setprio 3\n\t" \
  "v_minimum3_f32 v107, v107, v108, v109\n\t" \
  "v_minimum3_f32 v107, v107, v110, v111\n\t" \
  "v_minimum3_f32 v107, v107, v120, v121\n\t" \
  "v_minimum3_f32 v107, v107, v122, v123\n\t" \
  "v_cmp_nlt_f32 vcc, 0x03aa2425, v107\n\t" \
  "v_med3_i32 v64, %[da], %[lo], %[hi]\n\t" \
  "v_med3_i32 v65, %[da], v101, v102\n\t" \
  "v_med3_i32 v66, %[da], v103, v104\n\t" \
  "v_med3_i32 v67, %[da], v105, v106\n\t" \
  "ds_read_b64_tr_b16 v[80:81], v64 offset:0\n\t" \
  "ds_read_b64_tr_b16 v[82:83], v65 offset:64\n\t" \
  "ds_read_b64_tr_b16 v[84:85], v66 offset:128\n\t" \
  "ds_read_b64_tr_b16 v[86:87], v67 offset:192\n\t" \
  "v_add_u32 %[da], 0x100, %[da]\n\t" \
  "s_waitcnt vmcnt(0)\n\t" \
  "s_cbranch_vccnz 9f\n\t" \
  "ds_read_b64_tr_b16 v[32:33], %[cb] offset:0\n\t" \
  "ds_read_b64_tr_b16 v[34:35], %[cb] offset:2176\n\t" \
  "ds_read_b64_tr_b16 v[36:37], %[cb] offset:256\n\t" \
  "ds_read_b64_tr_b16 v[38:39], %[cb] offset:2432\n\t" \
  "ds_read_b64_tr_b16 v[40:41], %[cb] offset:512\n\t" \
  "ds_read_b64_tr_b16 v[42:43], %[cb] offset:2688\n\t" \
  "ds_read_b64_tr_b16 v[44:45], %[cb] offset:768\n\t" \
  "ds_read_b64_tr_b16 v[46:47], %[cb] offset:2944\n\t" \
  "ds_read_b64_tr_b16 v[48:49], %[cb] offset:64\n\t" \
  "ds_read_b64_tr_b16 v[50:51], %[cb] offset:2240\n\t" \
  "ds_read_b64_tr_b16 v[52:53], %[cb] offset:320\n\t" \
  "ds_read_b64_tr_b16 v[54:55], %[cb] offset:2496\n\t" \
  "ds_read_b64_tr_b16 v[56:57], %[cb] offset:576\n\t" \
  "ds_read_b64_tr_b16 v[58:59], %[cb] offset:2752\n\t" \
  "ds_read_b64_tr_b16 v[60:61], %[cb] offset:832\n\t" \
  "ds_read_b64_tr_b16 v[62:63], %[cb] offset:3008\n\t" \
  "s_waitcnt lgkmcnt(8)\n\t" \
  "v_mfma_f32_32x32x16_bf16 v[32:47], v[88:91], %[wf0], v[32:47]\n\t" \
  "s_sub_u32 %[rem], %[rem], 1\n\t" \
  "s_cmp_eq_u32 %[rem], 0\n\t" \
  "s_waitcnt lgkmcnt(0)\n\t" \
  "v_mfma_f32_32x32x16_bf16 v[48:63], v[88:91], %[wf1], v[48:63]\n\t" \
  "s_cbranch_scc1 2f\n\t" \
  "s_mov_b32 m0, %[stg]\n\t" \
  "s_nop 0\n\t" \
  "buffer_load_dwordx4 v[92:93], %[srd], 0 idxen offen sc1 lds\n\t" \
  "s_add_u32 m0, %[stg], 1088\n\t" \
  "s_nop 0\n\t" \
  "buffer_load_dwordx4 v[94:95], %[srd], 0 idxen offen sc1 lds\n\t" \
  "s_add_u32 m0, %[stg], 2176\n\t" \
  "s_nop 0\n\t" \
  "buffer_load_dwordx4 v[96:97], %[srd], 0 idxen offen sc1 lds\n\t" \
  "s_add_u32 m0, %[stg], 3264\n\t" \
  "s_nop 0\n\t" \
  "buffer_load_dwordx4 v[98:99], %[srd], 0 idxen offen sc1 lds\n\t" \
  "v_bfi_b32 v64, %[mh], v100, %[s1a]\n\t" \
  "ds_read_b64 v[88:89], v100 offset:244\n\t" \
  "ds_read_b64 v[90:91], v64 offset:252\n\t" \
  "ds_read_u16 v100, %[s1a] offset:512\n\t" \
  "ds_read_u16 v92, %[s4a] offset:512\n\t" \
  "ds_read_u16 v94, %[s4a] offset:520\n\t" \
  "ds_read_u16 v96, %[s4a] offset:528\n\t" \
  "ds_read_u16 v98, %[s4a] offset:536\n\t" \
  "v_add_u32 %[s1a], 0x100, %[s1a]\n\t" \
  "v_add_u32 %[s4a], 0x100, %[s4a]\n\t" \
  "s_setprio 2\n\t" \
  "s_cmp_lg_u32 %[rem], 1\n\t" \
  "s_cbranch_scc1 3f\n\t" \
  "s_mov_b32 m0, %[whd]\n\t" \
  "s_setprio 1\n\t" \
  "global_load_lds_dwordx4 %[whv], %[wh]\n\t" \
  "s_mov_b32 m0, %[whd2]\n\t" \
  "s_nop 0\n\t" \
  "global_load_lds_dwordx4 %[whv], %[wh2]\n\t" \
  "s_branch 3f\n\t" \
  "2:\n\t" \
  "v_mov_b32 v64, %[syw]\n\t" \
  "v_mov_b32 v65, 1\n\t" \
  "s_mov_b64 exec, 1\n\t" \
  "ds_add_u32 v64, v65\n\t" \
  "s_mov_b64 exec, -1\n\t" \
  "s_nop 3\n\t" \
  "s_setprio 0\n\t" \
  ".p2align 3\n\t" \
  "3:\n\t" \
  "v_exp_f32 v64, v32\n\t" \
  "v_exp_f32 v65, v33\n\t" \
  "v_exp_f32 v66, v34\n\t" \
  "v_exp_f32 v67, v35\n\t" \
  "v_exp_f32 v68, v36\n\t" \
  "v_exp_f32 v69, v37\n\t" \
  "v_exp_f32 v70, v38\n\t" \
  "v_exp_f32 v71, v39\n\t" \
  "v_exp_f32 v72, v40\n\t" \
  "v_exp_f32 v73, v41\n\t" \
  "v_exp_f32 v74, v42\n\t" \
  "v_exp_f32 v75, v43\n\t" \
  "v_exp_f32 v76, v44\n\t" \
  "v_exp_f32 v77, v45\n\t" \
  "v_exp_f32 v78, v46\n\t" \
  "v_exp_f32 v79, v47\n\t" \
  "v_pk_add_f32 v[64:65], v[64:65], 1.0 op_sel_hi:[1,0]\n\t" \
  "v_pk_add_f32 v[68:69], v[68:69], 1.0 op_sel_hi:[1,0]\n\t" \
  "v_pk_add_f32 v[72:73], v[72:73], 1.0 op_sel_hi:[1,0]\n\t" \
  "v_pk_add_f32 v[76:77], v[76:77], 1.0 op_sel_hi:[1,0]\n\t" \
  "v_pk_fma_f32 v[112:113], v[64:65], v[66:67], v[64:65]\n\t" \
  "v_pk_fma_f32 v[114:115], v[68:69], v[70:71], v[68:69]\n\t" \
  "v_pk_fma_f32 v[116:117], v[72:73], v[74:75], v[72:73]\n\t" \
  "v_pk_fma_f32 v[118:119], v[76:77], v[78:79], v[76:77]\n\t" \
  "v_pk_mul_f32 v[108:109], v[112:113], v[114:115]\n\t" \
  "v_pk_mul_f32 v[110:111], v[116:117], v[118:119]\n\t" \
  "v_rcp_f32 v108, v108\n\t" \
  "v_rcp_f32 v109, v109\n\t" \
  "v_rcp_f32 v110, v110\n\t" \
  "v_rcp_f32 v111, v111\n\t" \
  "v_pk_mul_f32 v[114:115], v[114:115], v[108:109]\n\t" \
  "v_pk_mul_f32 v[112:113], v[112:113], v[108:109]\n\t" \
  "v_pk_mul_f32 v[118:119], v[118:119], v[110:111]\n\t" \
  "v_pk_mul_f32 v[116:117], v[116:117], v[110:111]\n\t" \
  "v_pk_fma_f32 v[66:67], v[66:67], v[114:115], v[114:115]\n\t" \
  "v_pk_mul_f32 v[64:65], v[64:65], v[114:115]\n\t" \
  "v_pk_fma_f32 v[70:71], v[70:71], v[112:113], v[112:113]\n\t" \
  "v_pk_mul_f32 v[68:69], v[68:69], v[112:113]\n\t" \
  "v_pk_fma_f32 v[74:75], v[74:75], v[118:119], v[118:119]\n\t" \
  "v_pk_mul_f32 v[72:73], v[72:73], v[118:119]\n\t" \
  "v_pk_fma_f32 v[78:79], v[78:79], v[116:117], v[116:117]\n\t" \
  "v_pk_mul_f32 v[76:77], v[76:77], v[116:117]\n\t" \
  "v_pk_mul_f32 v[32:33], v[32:33], v[66:67]\n\t" \
  "v_pk_mul_f32 v[34:35], v[34:35], v[64:65]\n\t" \
  "v_pk_mul_f32 v[36:37], v[36:37], v[70:71]\n\t" \
  "v_pk_mul_f32 v[38:39], v[38:39], v[68:69]\n\t" \
  "v_pk_mul_f32 v[40:41], v[40:41], v[74:75]\n\t" \
  "v_pk_mul_f32 v[42:43], v[42:43], v[72:73]\n\t" \
  "v_pk_mul_f32 v[44:45], v[44:45], v[78:79]\n\t" \
  "v_pk_mul_f32 v[46:47], v[46:47], v[76:77]\n\t" \
  "v_cvt_pk_bf16_f32 v32, v32, v33\n\t" \
  "v_cvt_pk_bf16_f32 v33, v34, v35\n\t" \
  "v_cvt_pk_bf16_f32 v34, v36, v37\n\t" \
  "v_cvt_pk_bf16_f32 v35, v38, v39\n\t" \
  "v_cvt_pk_bf16_f32 v36, v40, v41\n\t" \
  "v_cvt_pk_bf16_f32 v37, v42, v43\n\t" \
  "v_cvt_pk_bf16_f32 v38, v44, v45\n\t" \
  "v_cvt_pk_bf16_f32 v39, v46, v47\n\t" \
  "v_exp_f32 v64, v48\n\t" \
  "v_exp_f32 v65, v49\n\t" \
  "v_mfma_f32_32x32x16_bf16 v[0:15], v[80:83], v[32:35], v[0:15]\n\t" \
  "v_exp_f32 v66, v50\n\t" \
  "v_exp_f32 v67, v51\n\t" \
  "v_exp_f32 v68, v52\n\t" \
  "v_exp_f32 v69, v53\n\t" \
  "v_mfma_f32_32x32x16_bf16 v[0:15], v[84:87], v[36:39], v[0:15]\n\t" \
  "v_exp_f32 v70, v54\n\t" \
  "v_exp_f32 v71, v55\n\t" \
  "v_exp_f32 v72, v56\n\t" \
  "v_exp_f32 v73, v57\n\t" \
  "v_exp_f32 v74, v58\n\t" \
  "v_exp_f32 v75, v59\n\t" \
  "v_exp_f32 v76, v60\n\t" \
  "v_exp_f32 v77, v61\n\t" \
  "v_exp_f32 v78, v62\n\t" \
  "v_exp_f32 v79, v63\n\t" \
  "v_pk_add_f32 v[64:65], v[64:65], 1.0 op_sel_hi:[1,0]\n\t" \
  "v_pk_add_f32 v[68:69], v[68:69], 1.0 op_sel_hi:[1,0]\n\t" \
  "v_pk_add_f32 v[72:73], v[72:73], 1.0 op_sel_hi:[1,0]\n\t" \
  "v_pk_add_f32 v[76:77], v[76:77], 1.0 op_sel_hi:[1,0]\n\t" \
  "v_pk_fma_f32 v[112:113], v[64:65], v[66:67], v[64:65]\n\t" \
  "v_pk_fma_f32 v[114:115], v[68:69], v[70:71], v[68:69]\n\t" \
  "v_pk_fma_f32 v[116:117], v[72:73], v[74:75], v[72:73]\n\t" \
  "v_pk_fma_f32 v[118:119], v[76:77], v[78:79], v[76:77]\n\t" \
  "v_pk_mul_f32 v[120:121], v[112:113], v[114:115]\n\t" \
  "v_pk_mul_f32 v[122:123], v[116:117], v[118:119]\n\t" \
  "v_rcp_f32 v120, v120\n\t" \
  "v_rcp_f32 v121, v121\n\t" \
  "v_rcp_f32 v122, v122\n\t" \
  "v_rcp_f32 v123, v123\n\t" \
  "v_pk_mul_f32 v[114:115], v[114:115], v[120:121]\n\t" \
  "v_pk_mul_f32 v[112:113], v[112:113], v[120:121]\n\t" \
  "v_pk_mul_f32 v[118:119], v[118:119], v[122:123]\n\t" \
  "v_pk_mul_f32 v[116:117], v[116:117], v[122:123]\n\t" \
  "v_pk_fma_f32 v[66:67], v[66:67], v[114:115], v[114:115]\n\t" \
  "v_pk_mul_f32 v[64:65], v[64:65], v[114:115]\n\t" \
  "v_pk_fma_f32 v[70:71], v[70:71], v[112:113], v[112:113]\n\t" \
  "v_pk_mul_f32 v[68:69], v[68:69], v[112:113]\n\t" \
  "v_pk_fma_f32 v[74:75], v[74:75], v[118:119], v[118:119]\n\t" \
  "v_pk_mul_f32 v[72:73], v[72:73], v[118:119]\n\t" \
  "v_pk_fma_f32 v[78:79], v[78:79], v[116:117], v[116:117]\n\t" \
  "v_pk_mul_f32 v[76:77], v[76:77], v[116:117]\n\t" \
  "v_pk_mul_f32 v[48:49], v[48:49], v[66:67]\n\t" \
  "v_pk_mul_f32 v[50:51], v[50:51], v[64:65]\n\t" \
  "v_pk_mul_f32 v[52:53], v[52:53], v[70:71]\n\t" \
  "v_pk_mul_f32 v[54:55], v[54:55], v[68:69]\n\t" \
  "v_pk_mul_f32 v[56:57], v[56:57], v[74:75]\n\t" \
  "v_pk_mul_f32 v[58:59], v[58:59], v[72:73]\n\t" \
  "v_pk_mul_f32 v[60:61], v[60:61], v[78:79]\n\t" \
  "v_pk_mul_f32 v[62:63], v[62:63], v[76:77]\n\t" \
  "v_cvt_pk_bf16_f32 v48, v48, v49\n\t" \
  "v_cvt_pk_bf16_f32 v49, v50, v51\n\t" \
  "v_cvt_pk_bf16_f32 v50, v52, v53\n\t" \
  "v_cvt_pk_bf16_f32 v51, v54, v55\n\t" \
  "v_cvt_pk_bf16_f32 v52, v56, v57\n\t" \
  "v_cvt_pk_bf16_f32 v53, v58, v59\n\t" \
  "v_cvt_pk_bf16_f32 v54, v60, v61\n\t" \
  "v_cvt_pk_bf16_f32 v55, v62, v63\n\t" \
  "v_mfma_f32_32x32x16_bf16 v[16:31], v[80:83], v[48:51], v[16:31]\n\t" \
  "s_cmp_lg_u32 %[rem], 0\n\t" \
  "v_mfma_f32_32x32x16_bf16 v[16:31], v[84:87], v[52:55], v[16:31]\n\t" \
  "s_cbranch_scc1 1b\n\t" \
  "v_minimum3_f32 v107, v107, v108, v109\n\t" \
  "v_minimum3_f32 v107, v107, v110, v111\n\t" \
  "v_minimum3_f32 v107, v107, v120, v121\n\t" \
  "v_minimum3_f32 v107, v107, v122, v123\n\t" \
  "9:\n\t" \
  "s_waitcnt lgkmcnt(0)\n\t" \
  "s_nop 11\n\t"
#define TILE_LOOP_SAFE \
  "ds_read_u16 v100, %[s1a] offset:256\n\t" \
  "v_mov_b32 v93, %[lp]\n\t" \
  "v_mov_b32 v95, %[lp]\n\t" \
  "v_mov_b32 v97, %[lp]\n\t" \
  "v_mov_b32 v99, %[lp]\n\t" \
  "v_subrev_u32 v101, 64, %[lo]\n\t" \
  "v_subrev_u32 v102, 64, %[hi]\n\t" \
  "v_subrev_u32 v103, 128, %[lo]\n\t" \
  "v_subrev_u32 v104, 128, %[hi]\n\t" \
  "v_subrev_u32 v105, 192, %[lo]\n\t" \
  "v_subrev_u32 v106, 192, %[hi]\n\t" \
  "v_mov_b32 v107, 1.0\n\t" \
  "s_waitcnt lgkmcnt(0)\n\t" \
  "v_bfi_b32 v64, %[mh], v100, %[s1a]\n\t" \
  "ds_read_b64 v[88:89], v100 offset:244\n\t" \
  "ds_read_b64 v[90:91], v64 offset:252\n\t" \
  "ds_read_u16 v100, %[s1a] offset:512\n\t" \
  "ds_read_u16 v92, %[s4a] offset:512\n\t" \
  "ds_read_u16 v94, %[s4a] offset:520\n\t" \
  "ds_read_u16 v96, %[s4a] offset:528\n\t" \
  "ds_read_u16 v98, %[s4a] offset:536\n\t" \
  "v_add_u32 %[s1a], 0x100, %[s1a]\n\t" \
  "v_add_u32 %[s4a], 0x100, %[s4a]\n\t" \
  "1:\n\t" \
  "s_setprio 3\n\t" \
  "v_med3_i32 v64, %[da], %[lo], %[hi]\n\t" \
  "v_med3_i32 v65, %[da], v101, v102\n\t" \
  "v_med3_i32 v66, %[da], v103, v104\n\t" \
  "v_med3_i32 v67, %[da], v105, v106\n\t" \
  "ds_read_b64_tr_b16 v[80:81], v64 offset:0\n\t" \
  "ds_read_b64_tr_b16 v[82:83], v65 offset:64\n\t" \
  "ds_read_b64_tr_b16 v[84:85], v66 offset:128\n\t" \
  "ds_read_b64_tr_b16 v[86:87], v67 offset:192\n\t" \
  "v_add_u32 %[da], 0x100, %[da]\n\t" \
  "s_waitcnt vmcnt(0)\n\t" \
  "ds_read_b64_tr_b16 v[32:33], %[cb] offset:0\n\t" \
  "ds_read_b64_tr_b16 v[34:35], %[cb] offset:2176\n\t" \
  "ds_read_b64_tr_b16 v[36:37], %[cb] offset:256\n\t" \
  "ds_read_b64_tr_b16 v[38:39], %[cb] offset:2432\n\t" \
  "ds_read_b64_tr_b16 v[40:41], %[cb] offset:512\n\t" \
  "ds_read_b64_tr_b16 v[42:43], %[cb] offset:2688\n\t" \
  "ds_read_b64_tr_b16 v[44:45], %[cb] offset:768\n\t" \
  "ds_read_b64_tr_b16 v[46:47], %[cb] offset:2944\n\t" \
  "ds_read_b64_tr_b16 v[48:49], %[cb] offset:64\n\t" \
  "ds_read_b64_tr_b16 v[50:51], %[cb] offset:2240\n\t" \
  "ds_read_b64_tr_b16 v[52:53], %[cb] offset:320\n\t" \
  "ds_read_b64_tr_b16 v[54:55], %[cb] offset:2496\n\t" \
  "ds_read_b64_tr_b16 v[56:57], %[cb] offset:576\n\t" \
  "ds_read_b64_tr_b16 v[58:59], %[cb] offset:2752\n\t" \
  "ds_read_b64_tr_b16 v[60:61], %[cb] offset:832\n\t" \
  "ds_read_b64_tr_b16 v[62:63], %[cb] offset:3008\n\t" \
  "s_waitcnt lgkmcnt(8)\n\t" \
  "v_mfma_f32_32x32x16_bf16 v[32:47], v[88:91], %[wf0], v[32:47]\n\t" \
  "s_sub_u32 %[rem], %[rem], 1\n\t" \
  "s_cmp_eq_u32 %[rem], 0\n\t" \
  "s_waitcnt lgkmcnt(0)\n\t" \
  "v_mfma_f32_32x32x16_bf16 v[48:63], v[88:91], %[wf1], v[48:63]\n\t" \
  "s_cbranch_scc1 2f\n\t" \
  "s_mov_b32 m0, %[stg]\n\t" \
  "s_nop 0\n\t" \
  "buffer_load_dwordx4 v[92:93], %[srd], 0 idxen offen sc1 lds\n\t" \
  "s_add_u32 m0, %[stg], 1088\n\t" \
  "s_nop 0\n\t" \
  "buffer_load_dwordx4 v[94:95], %[srd], 0 idxen offen sc1 lds\n\t" \
  "s_add_u32 m0, %[stg], 2176\n\t" \
  "s_nop 0\n\t" \
  "buffer_load_dwordx4 v[96:97], %[srd], 0 idxen offen sc1 lds\n\t" \
  "s_add_u32 m0, %[stg], 3264\n\t" \
  "s_nop 0\n\t" \
  "buffer_load_dwordx4 v[98:99], %[srd], 0 idxen offen sc1 lds\n\t" \
  "v_bfi_b32 v64, %[mh], v100, %[s1a]\n\t" \
  "ds_read_b64 v[88:89], v100 offset:244\n\t" \
  "ds_read_b64 v[90:91], v64 offset:252\n\t" \
  "ds_read_u16 v100, %[s1a] offset:512\n\t" \
  "ds_read_u16 v92, %[s4a] offset:512\n\t" \
  "ds_read_u16 v94, %[s4a] offset:520\n\t" \
  "ds_read_u16 v96, %[s4a] offset:528\n\t" \
  "ds_read_u16 v98, %[s4a] offset:536\n\t" \
  "v_add_u32 %[s1a], 0x100, %[s1a]\n\t" \
  "v_add_u32 %[s4a], 0x100, %[s4a]\n\t" \
  "s_setprio 2\n\t" \
  "s_cmp_lg_u32 %[rem], 1\n\t" \
  "s_cbranch_scc1 3f\n\t" \
  "s_mov_b32 m0, %[whd]\n\t" \
  "s_setprio 1\n\t" \
  "global_load_lds_dwordx4 %[whv], %[wh]\n\t" \
  "s_mov_b32 m0, %[whd2]\n\t" \
  "s_nop 0\n\t" \
  "global_load_lds_dwordx4 %[whv], %[wh2]\n\t" \
  "s_branch 3f\n\t" \
  "2:\n\t" \
  "v_mov_b32 v64, %[syw]\n\t" \
  "v_mov_b32 v65, 1\n\t" \
  "s_mov_b64 exec, 1\n\t" \
  "ds_add_u32 v64, v65\n\t" \
  "s_mov_b64 exec, -1\n\t" \
  "s_nop 3\n\t" \
  "s_setprio 0\n\t" \
  ".p2align 3\n\t" \
  "3:\n\t" \
  "v_exp_f32 v64, v32\n\t" \
  "v_exp_f32 v65, v33\n\t" \
  "v_exp_f32 v66, v34\n\t" \
  "v_exp_f32 v67, v35\n\t" \
  "v_exp_f32 v68, v36\n\t" \
  "v_exp_f32 v69, v37\n\t" \
  "v_exp_f32 v70, v38\n\t" \
  "v_exp_f32 v71, v39\n\t" \
  "v_exp_f32 v72, v40\n\t" \
  "v_exp_f32 v73, v41\n\t" \
  "v_exp_f32 v74, v42\n\t" \
  "v_exp_f32 v75, v43\n\t" \
  "v_exp_f32 v76, v44\n\t" \
  "v_exp_f32 v77, v45\n\t" \
  "v_exp_f32 v78, v46\n\t" \
  "v_exp_f32 v79, v47\n\t" \
  "v_pk_add_f32 v[64:65], v[64:65], 1.0 op_sel_hi:[1,0]\n\t" \
  "v_pk_add_f32 v[66:67], v[66:67], 1.0 op_sel_hi:[1,0]\n\t" \
  "v_pk_add_f32 v[68:69], v[68:69], 1.0 op_sel_hi:[1,0]\n\t" \
  "v_pk_add_f32 v[70:71], v[70:71], 1.0 op_sel_hi:[1,0]\n\t" \
  "v_pk_add_f32 v[72:73], v[72:73], 1.0 op_sel_hi:[1,0]\n\t" \
  "v_pk_add_f32 v[74:75], v[74:75], 1.0 op_sel_hi:[1,0]\n\t" \
  "v_pk_add_f32 v[76:77], v[76:77], 1.0 op_sel_hi:[1,0]\n\t" \
  "v_pk_add_f32 v[78:79], v[78:79], 1.0 op_sel_hi:[1,0]\n\t" \
  "v_rcp_f32 v64, v64\n\t" \
  "v_rcp_f32 v65, v65\n\t" \
  "v_rcp_f32 v66, v66\n\t" \
  "v_rcp_f32 v67, v67\n\t" \
  "v_rcp_f32 v68, v68\n\t" \
  "v_rcp_f32 v69, v69\n\t" \
  "v_rcp_f32 v70, v70\n\t" \
  "v_rcp_f32 v71, v71\n\t" \
  "v_rcp_f32 v72, v72\n\t" \
  "v_rcp_f32 v73, v73\n\t" \
  "v_rcp_f32 v74, v74\n\t" \
  "v_rcp_f32 v75, v75\n\t" \
  "v_rcp_f32 v76, v76\n\t" \
  "v_rcp_f32 v77, v77\n\t" \
  "v_rcp_f32 v78, v78\n\t" \
  "v_rcp_f32 v79, v79\n\t" \
  "v_pk_mul_f32 v[32:33], v[32:33], v[64:65]\n\t" \
  "v_pk_mul_f32 v[34:35], v[34:35], v[66:67]\n\t" \
  "v_pk_mul_f32 v[36:37], v[36:37], v[68:69]\n\t" \
  "v_pk_mul_f32 v[38:39], v[38:39], v[70:71]\n\t" \
  "v_pk_mul_f32 v[40:41], v[40:41], v[72:73]\n\t" \
  "v_pk_mul_f32 v[42:43], v[42:43], v[74:75]\n\t" \
  "v_pk_mul_f32 v[44:45], v[44:45], v[76:77]\n\t" \
  "v_pk_mul_f32 v[46:47], v[46:47], v[78:79]\n\t" \
  "v_cvt_pk_bf16_f32 v32, v32, v33\n\t" \
  "v_cvt_pk_bf16_f32 v33, v34, v35\n\t" \
  "v_cvt_pk_bf16_f32 v34, v36, v37\n\t" \
  "v_cvt_pk_bf16_f32 v35, v38, v39\n\t" \
  "v_cvt_pk_bf16_f32 v36, v40, v41\n\t" \
  "v_cvt_pk_bf16_f32 v37, v42, v43\n\t" \
  "v_cvt_pk_bf16_f32 v38, v44, v45\n\t" \
  "v_cvt_pk_bf16_f32 v39, v46, v47\n\t" \
  "v_exp_f32 v64, v48\n\t" \
  "v_exp_f32 v65, v49\n\t" \
  "v_mfma_f32_32x32x16_bf16 v[0:15], v[80:83], v[32:35], v[0:15]\n\t" \
  "v_exp_f32 v66, v50\n\t" \
  "v_exp_f32 v67, v51\n\t" \
  "v_exp_f32 v68, v52\n\t" \
  "v_exp_f32 v69, v53\n\t" \
  "v_mfma_f32_32x32x16_bf16 v[0:15], v[84:87], v[36:39], v[0:15]\n\t" \
  "v_exp_f32 v70, v54\n\t" \
  "v_exp_f32 v71, v55\n\t" \
  "v_exp_f32 v72, v56\n\t" \
  "v_exp_f32 v73, v57\n\t" \
  "v_exp_f32 v74, v58\n\t" \
  "v_exp_f32 v75, v59\n\t" \
  "v_exp_f32 v76, v60\n\t" \
  "v_exp_f32 v77, v61\n\t" \
  "v_exp_f32 v78, v62\n\t" \
  "v_exp_f32 v79, v63\n\t" \
  "v_pk_add_f32 v[64:65], v[64:65], 1.0 op_sel_hi:[1,0]\n\t" \
  "v_pk_add_f32 v[66:67], v[66:67], 1.0 op_sel_hi:[1,0]\n\t" \
  "v_pk_add_f32 v[68:69], v[68:69], 1.0 op_sel_hi:[1,0]\n\t" \
  "v_pk_add_f32 v[70:71], v[70:71], 1.0 op_sel_hi:[1,0]\n\t" \
  "v_pk_add_f32 v[72:73], v[72:73], 1.0 op_sel_hi:[1,0]\n\t" \
  "v_pk_add_f32 v[74:75], v[74:75], 1.0 op_sel_hi:[1,0]\n\t" \
  "v_pk_add_f32 v[76:77], v[76:77], 1.0 op_sel_hi:[1,0]\n\t" \
  "v_pk_add_f32 v[78:79], v[78:79], 1.0 op_sel_hi:[1,0]\n\t" \
  "v_rcp_f32 v64, v64\n\t" \
  "v_rcp_f32 v65, v65\n\t" \
  "v_rcp_f32 v66, v66\n\t" \
  "v_rcp_f32 v67, v67\n\t" \
  "v_rcp_f32 v68, v68\n\t" \
  "v_rcp_f32 v69, v69\n\t" \
  "v_rcp_f32 v70, v70\n\t" \
  "v_rcp_f32 v71, v71\n\t" \
  "v_rcp_f32 v72, v72\n\t" \
  "v_rcp_f32 v73, v73\n\t" \
  "v_rcp_f32 v74, v74\n\t" \
  "v_rcp_f32 v75, v75\n\t" \
  "v_rcp_f32 v76, v76\n\t" \
  "v_rcp_f32 v77, v77\n\t" \
  "v_rcp_f32 v78, v78\n\t" \
  "v_rcp_f32 v79, v79\n\t" \
  "v_pk_mul_f32 v[48:49], v[48:49], v[64:65]\n\t" \
  "v_pk_mul_f32 v[50:51], v[50:51], v[66:67]\n\t" \
  "v_pk_mul_f32 v[52:53], v[52:53], v[68:69]\n\t" \
  "v_pk_mul_f32 v[54:55], v[54:55], v[70:71]\n\t" \
  "v_pk_mul_f32 v[56:57], v[56:57], v[72:73]\n\t" \
  "v_pk_mul_f32 v[58:59], v[58:59], v[74:75]\n\t" \
  "v_pk_mul_f32 v[60:61], v[60:61], v[76:77]\n\t" \
  "v_pk_mul_f32 v[62:63], v[62:63], v[78:79]\n\t" \
  "v_cvt_pk_bf16_f32 v48, v48, v49\n\t" \
  "v_cvt_pk_bf16_f32 v49, v50, v51\n\t" \
  "v_cvt_pk_bf16_f32 v50, v52, v53\n\t" \
  "v_cvt_pk_bf16_f32 v51, v54, v55\n\t" \
  "v_cvt_pk_bf16_f32 v52, v56, v57\n\t" \
  "v_cvt_pk_bf16_f32 v53, v58, v59\n\t" \
  "v_cvt_pk_bf16_f32 v54, v60, v61\n\t" \
  "v_cvt_pk_bf16_f32 v55, v62, v63\n\t" \
  "v_mfma_f32_32x32x16_bf16 v[16:31], v[80:83], v[48:51], v[16:31]\n\t" \
  "s_cmp_lg_u32 %[rem], 0\n\t" \
  "v_mfma_f32_32x32x16_bf16 v[16:31], v[84:87], v[52:55], v[16:31]\n\t" \
  "s_cbranch_scc1 1b\n\t" \
  "9:\n\t" \
  "s_waitcnt lgkmcnt(0)\n\t" \
  "s_nop 11\n\t"
#define TILE_LOOP_CLOBBERS "v32", "v33", "v34", "v35", "v36", "v37", "v38", "v39", "v40", "v41", "v42", "v43", "v44", "v45", "v46", "v47", "v48", "v49", "v50", "v51", "v52", "v53", "v54", "v55", "v56", "v57", "v58", "v59", "v60", "v61", "v62", "v63", "v64", "v65", "v66", "v67", "v68", "v69", "v70", "v71", "v72", "v73", "v74", "v75", "v76", "v77", "v78", "v79", "v80", "v81", "v82", "v83", "v84", "v85", "v86", "v87", "v88", "v89", "v90", "v91", "v92", "v93", "v94", "v95", "v96", "v97", "v98", "v99", "v100", "v101", "v102", "v103", "v104", "v105", "v106", "v108", "v109", "v110", "v111", "v112", "v113", "v114", "v115", "v116", "v117", "v118", "v119", "v120", "v121", "v122", "v123"

struct Win { u32 cbase, s4a, s1a, mh, lp, gcol, q, bp; int lo, hi, da; bf16x8 wf0, wf1; f32x16 acc0, acc1; };
DI void init_window(Win& W, int lane, u32 stg) {
  const int hh = lane >> 5, blk = (lane >> 4) & 1, q = (lane >> 2) & 3, p = lane & 3;
  const u32 real = stg + 128 * hh + 32 * blk + 8 * p + (q >> 1) * STGS, zero = L_ZERO + 128 * (1 - hh);
  W.cbase = (q & 1) ? real : zero;
  W.s4a = L_S - 256 + (u32)(4 * (lane >> 3)) * 8; W.s1a = L_S - 256 + (u32)(lane & 31) * 8 + 4 + 2 * hh;
  W.da = (int)L_DV + 8 * (4 * hh + q + 1);
  W.mh = hh ? ~0u : 0u; W.lp = (lane & 7) * 16;
  W.gcol = L_G + 32 * blk + 8 * p; W.q = q; W.bp = 4 * blk + p;
  asm volatile("v_mov_b64 v[0:1], 0\n\tv_mov_b64 v[2:3], 0\n\tv_mov_b64 v[4:5], 0\n\tv_mov_b64 v[6:7], 0\n\tv_mov_b64 v[8:9], 0\n\tv_mov_b64 v[10:11], 0\n\t"
               "v_mov_b64 v[12:13], 0\n\tv_mov_b64 v[14:15], 0\n\tv_mov_b64 v[16:17], 0\n\tv_mov_b64 v[18:19], 0\n\tv_mov_b64 v[20:21], 0\n\tv_mov_b64 v[22:23], 0\n\t"
               "v_mov_b64 v[24:25], 0\n\tv_mov_b64 v[26:27], 0\n\tv_mov_b64 v[28:29], 0\n\tv_mov_b64 v[30:31], 0"
               : "=&{v[0:15]}"(W.acc0), "=&{v[16:31]}"(W.acc1), "+v"(W.cbase), "+v"(W.s4a), "+v"(W.s1a), "+v"(W.mh), "+v"(W.lp), "+v"(W.da),
                 "+v"(W.gcol), "+v"(W.q), "+v"(W.bp));
}
DI void setup_window(Win& W, int nn, int base, u32 sbeg) {
  const u32 s8 = (sbeg & ~31u) * 8;
  W.s1a += s8;
  const u32 rb = (W.mh & (u32)base) | (~W.mh & 48u), t0 = (W.q - rb) & 7;
  const u32 a0 = W.gcol + (rb + t0) * 128, a1 = W.gcol + (rb + (t0 ^ 4)) * 128;
  W.wf0 = as_frag(tr_read(a0), tr_read(a1)); W.wf1 = as_frag(tr_read(a0 + 64), tr_read(a1 + 64));
  const int np = min(base + (int)((W.bp - base) & 7), nn);
  W.lo = (int)(L_DV - L_S) + (int)lds_ld<u32>(L_NST + np * 16) + 8 * np;
  W.hi = (int)(L_DV - L_S + 8) + (int)lds_ld<u32>(L_NST + np * 16 + 16) + 8 * np;
  W.da += (int)s8 + 8 * np;
  asm volatile("" : "+v"(W.s4a), "+v"(W.s1a), "+v"(W.lo), "+v"(W.hi), "+v"(W.da), "+v"(W.wf0), "+v"(W.wf1));
}
DI void first_tile(const Win& W, u32 stg, u32x4 srd) {
  asm volatile(TILE_FIRST ::[s4a] "v"(W.s4a), [lp] "v"(W.lp), [srd] "s"(srd), [stg] "s"(stg) : "memory", "scc", TILE_FIRST_CLOBBERS);
}
template <bool QUAD>
DI float run_tiles(Win& W, u32 stg, u32x4 srd, u32 sbeg, u32 wend, const GAS u16* wh, int w, u32 whv, u32 syw, u32& rem) {
  const int w2 = min(w + 12, 15);
  rem = ((wend + 31) >> 5) - (sbeg >> 5);
  float rmin;
  if (QUAD)
    asm volatile(TILE_LOOP_QUAD
                 : "+{v[0:15]}"(W.acc0), "+{v[16:31]}"(W.acc1), "=&{v107}"(rmin), [rem] "+s"(rem), [s4a] "+v"(W.s4a), [s1a] "+v"(W.s1a), [da] "+v"(W.da)
                 : [srd] "s"(srd), [stg] "s"(stg), [cb] "v"(W.cbase), [lp] "v"(W.lp), [mh] "v"(W.mh), [lo] "v"(W.lo), [hi] "v"(W.hi), [wf0] "v"(W.wf0),
                   [wf1] "v"(W.wf1), [syw] "s"(syw), [whv] "v"(whv), [wh] "s"(wh + w * 512), [wh2] "s"(wh + w2 * 512), [whd] "s"(L_WH + (u32)w * 1024),
                   [whd2] "s"(L_WH + (u32)w2 * 1024)
                 : "memory", "scc", "vcc", TILE_LOOP_CLOBBERS);
  else
    asm volatile(TILE_LOOP_SAFE
                 : "+{v[0:15]}"(W.acc0), "+{v[16:31]}"(W.acc1), "=&{v107}"(rmin), [rem] "+s"(rem), [s4a] "+v"(W.s4a), [s1a] "+v"(W.s1a), [da] "+v"(W.da)
                 : [srd] "s"(srd), [stg] "s"(stg), [cb] "v"(W.cbase), [lp] "v"(W.lp), [mh] "v"(W.mh), [lo] "v"(W.lo), [hi] "v"(W.hi), [wf0] "v"(W.wf0),
                   [wf1] "v"(W.wf1), [syw] "s"(syw), [whv] "v"(whv), [wh] "s"(wh + w * 512), [wh2] "s"(wh + w2 * 512), [whd] "s"(L_WH + (u32)w * 1024),
                   [whd2] "s"(L_WH + (u32)w2 * 1024)
                 : "memory", "scc", "vcc", TILE_LOOP_CLOBBERS);
  return rmin;
}
DI void load_wh(const GAS u16* wh, int w, int lane) {
  dma16(wh, (u32)(w * 1024 + lane * 16), L_WH + (u32)w * 1024);
  if (w < 4) dma16(wh, (u32)((w + 12) * 1024 + lane * 16), L_WH + (u32)(w + 12) * 1024);
}
struct Node { f32x4 t[2]; bf16x8 wh2[2][2]; f32x2 hr[4]; };
DI void node_hpart(Node& nd, u32 ha, u32 hra, u32 wa, u32 wa2) {
  const u32x4 hf0 = lds_ld<u32x4>(ha), hf1 = lds_ld<u32x4>(ha + 64), hr = lds_ld<u32x4>(hra);
#pragma unroll
  for (int T = 0; T < 2; ++T) {
    f32x4 z = {0.f, 0.f, 0.f, 0.f};
    z = MFMA16(as_frag(tr_read(wa + 8 * T), tr_read(wa + 8 * T + 512)), as_frag(hf0), z);
    z = MFMA16(as_frag(tr_read(wa + 8 * T + 4096), tr_read(wa + 8 * T + 4608)), as_frag(hf1), z);
    nd.t[T] = z * NLOG2E;
    nd.wh2[T][0] = as_frag(tr_read(wa2 + 8 * T), tr_read(wa2 + 8 * T + 256));
    nd.wh2[T][1] = as_frag(tr_read(wa2 + 8 * T + 2048), tr_read(wa2 + 8 * T + 2304));
  }
#pragma unroll
  for (int i = 0; i < 4; ++i) nd.hr[i] = f32x2{b2f(hr[i]), bhi(hr[i])};
  asm volatile("" : "+v"(nd.t[0]), "+v"(nd.t[1]), "+v"(nd.wh2[0][0]), "+v"(nd.wh2[0][1]), "+v"(nd.wh2[1][0]), "+v"(nd.wh2[1][1]), "+v"(nd.hr[0]), "+v"(nd.hr[1]),
               "+v"(nd.hr[2]), "+v"(nd.hr[3]));
}
DI void node_msg(Node& nd, u32x4 m0, u32x4 m1) {
#pragma unroll
  for (int T = 0; T < 2; ++T) nd.t[T] = MFMA16(nd.wh2[T][0], as_frag(m0), nd.t[T]);
#pragma unroll
  for (int T = 0; T < 2; ++T) nd.t[T] = MFMA16(nd.wh2[T][1], as_frag(m1), nd.t[T]);
}
DI void put_node(const Node& nd, u32x4 osrd, u32 oo) {
  u32x4 ov;
#pragma unroll
  for (int i = 0; i < 4; ++i) {
    const f32x2 t = {nd.t[i >> 1][2 * (i & 1)], nd.t[i >> 1][2 * (i & 1) + 1]};
    const f32x2 e = f32x2{__builtin_amdgcn_exp2f(t[0]), __builtin_amdgcn_exp2f(t[1])} + 1.0f;
    const f32x2 o = (t * NLN2) * f32x2{__builtin_amdgcn_rcpf(e[0]), __builtin_amdgcn_rcpf(e[1])} + nd.hr[i];
    ov[i] = pk2(o[0], o[1]);
  }
  asm volatile("buffer_store_dwordx4 %0, %1, %2, 0 offen\n\ts_nop 1" ::"v"(ov), "v"(oo), "s"(osrd) : "memory");
}
DI f32x4 sum16(u32 a) { return (lds_ld<f32x4>(a) + lds_ld<f32x4>(a + 16)) + (lds_ld<f32x4>(a + 32) + lds_ld<f32x4>(a + 48)); }
DI void put_pos(f32x4 pp, float pold, u32x4 osrd, u32 oo) {
  float ps = (pp[0] + pp[1]) + (pp[2] + pp[3]);
  ps += dppf<0xB1>(ps);
  asm volatile("buffer_store_short %0, %1, %2, 0 offen" ::"v"(pk2(pold + ps * NLN2, 0.f)), "v"(oo), "s"(osrd) : "memory");
}

__global__ __launch_bounds__(NW * 64, 3) void egnn_fused(const u16* h_, const u16* pos_, const void* ea_, const void* src_, const void* dst_,
                                                      const u16* we_, const u16* wx_, const u16* wh_, char* ws_, u16* out_) {
  u64x8 ka; u64x2 kb;
  asm volatile("s_load_dwordx16 %0, %2, 0x0\n\ts_load_dwordx4 %1, %2, 0x40" : "=&s"(ka), "=&s"(kb) : "s"(__builtin_amdgcn_kernarg_segment_ptr()) : "memory");
  u64 pc;
  asm volatile("s_getpc_b64 %0" : "=s"(pc));
  const int tid = threadIdx.x;
  int lane = tid & 63;
  const int w = __builtin_amdgcn_readfirstlane(tid >> 6);
  const int c = blockIdx.x;
  const int b = (c & 7) * 32 + (c >> 3);
  const int n0 = first_node(b), nn = first_node(b + 1) - n0;
  const u32 stg = L_STG + (u32)w * STGW, dsc = L_D + (u32)w * DSTR;
  if (tid < CODE_LINES) asm volatile("s_mov_b32 m0, %2\n\ts_nop 0\n\tglobal_load_lds_dword %0, %1" ::"v"((u32)(tid * 128)), "s"(pc), "s"(L_WH + 12288) : "memory");
  {
    constexpr u32 NZ = (L_NST - L_ZERO) / 16;
    if (tid < NZ + 64) lds_st<u32x4>(tid < NZ ? L_ZERO + tid * 16 : L_HIST + (tid - NZ) * 16, u32x4{0, 0, 0, 0});
    if (tid < NW * DSTR / 16) lds_st<u32x4>(L_D + tid * 16, u32x4{0, 0, 0, 0});
    if (w == 10) lds_st<u32>(L_NST + 1024 + lane * 4, L_S + (CAP + 64) * 8);
    if (w == 11) lds_st<u32>(L_OH + lane * 4, (((lane >> 3) & 3) == (lane & 3)) ? (((lane >> 2) & 1) ? 0x3F800000u : 0x3F80u) : 0u);
  }
  asm volatile("s_waitcnt lgkmcnt(0)" ::: "memory");
  __builtin_amdgcn_sched_barrier(0);
  asm volatile("" : "+s"(ka), "+s"(kb));
  const GAS u16* h = (const GAS u16*)ka[0]; const GAS u16* pos = (const GAS u16*)ka[1];
  const GAS char* ea4 = (const GAS char*)ka[2]; const GAS char* src4 = (const GAS char*)ka[3]; const GAS char* dst4 = (const GAS char*)ka[4];
  const GAS u16* we = (const GAS u16*)ka[5]; const GAS u16* wx = (const GAS u16*)ka[6]; const GAS u16* wh = (const GAS u16*)ka[7];
  GAS char* ws = (GAS char*)kb[0];
  const u32 epoch = *(const __attribute__((address_space(4))) u32*)(kb[0] + WS_EP);
  const u32 tag = epoch + 0x9E3779B1u, tag8 = (tag * 0x01010101u) & 0xff000000u;
  const u64 ftag = ((u64)tag << 32) | ~tag, gtag = ((u64)tag << 8) | (tag8 >> 24);

  const bool act1 = w * 256 + 128 + 2 * lane < CH;
  u32x2 d0, d1, s0, s1;
  u32x4 e0, e1;
  {
    const u32 q0 = (u32)(c * CH + (w < 10 ? w * 256 + 2 * lane : 0)), q1 = act1 ? q0 + 128 : q0;
    asm volatile("global_load_dwordx2 %0, %4, %6 nt\n\tglobal_load_dwordx2 %1, %5, %6 nt\n\tglobal_load_dwordx2 %2, %4, %7 nt\n\tglobal_load_dwordx2 %3, %5, %7 nt"
                 : "=&v"(d0), "=&v"(d1), "=&v"(s0), "=&v"(s1) : "v"(q0 * 4), "v"(q1 * 4), "s"(dst4), "s"(src4) : "memory");
    asm volatile("s_barrier" ::: "memory");
    asm volatile("global_load_dwordx4 %0, %2, %4 nt\n\tglobal_load_dwordx4 %1, %3, %4 nt" : "=&v"(e0), "=&v"(e1) : "v"(q0 * 8), "v"(q1 * 8), "s"(ea4) : "memory");
  }
  if (w >= 10) {
    const int m = 11 - w;
#pragma unroll
    for (int i = 0; i < 3; ++i) dma16(h, umin((u32)(n0 * 128 + (3 * m + i) * 1024 + lane * 16), (u32)(NN * 128 - 16)), L_HR + (u32)(3 * m + i) * 1024);
    asm volatile("s_mov_b32 m0, %2\n\ts_nop 0\n\tglobal_load_lds_dword %0, %1" ::"v"(m ? umin((u32)((c >> 3) * 1920 + (lane & 31) * 64), (u32)(NN * 6 - 4)) : (u32)((c >> 3) * 512 + (lane & 7) * 64)),
                 "s"(m ? pos : wh), "s"(L_SINK) : "memory");
#pragma unroll
    for (int i = 0; i < 8; ++i) dma16(we, (u32)((8 * m + i) * 1024 + lane * 16), L_WE + (u32)(8 * m + i) * 1024);
  }
  Win W;
  init_window(W, lane, stg);
  const int seg0 = w * 21 + min(w, 4), nseg = w < 4 ? 22 : 21;
  const int seg = seg0 + lane;
  const bool sv = lane < nseg;
  const u64 svm = (1ull << nseg) - 1;
  u32 taba = (u32)(((sv ? seg : c) * NBK + 2 * (b >> 2) + (b & 1) + 128 * ((b >> 1) & 1)) * 8), flea = (u32)((sv ? seg : c) * 1024 + (c & 7) * 128);
  u32 segbase = (u32)seg * CHS * 8, rep4 = (lane & 3) * 4, dummy = 1024u + lane * 4;
  const int pw = w < 8 ? w - 6 : (w < 10 ? w - 4 : w - 8);
  u32 ha, hra, wa, wa2, oo, mo, mska, fa, fp;
  {
    const int etile = w >= 3 ? w - 3 : w, hv = w >= 3;
    const int nl = etile * 16 + (lane & 15), g = lane >> 4, pi = pw * 32 + (lane >> 1);
    const int pn = pi < 120 ? (pi * 43) >> 7 : 63, pc3 = pi - pn * 3;
    ha = L_HR + nl * 128 + g * 16;
    wa = L_WH + (u32)((8 * g + ((lane >> 2) & 3)) * 128 + 64 * hv + 16 * (lane & 3));
    wa2 = L_WH + (u32)((64 + 4 * g + ((lane >> 3) & 1) + 32 * ((lane >> 2) & 1)) * 128 + 64 * hv + 16 * (lane & 3));
    if (w < 6) {
      hra = ha + 64 * hv;
      oo = nl < nn ? (u32)((n0 + nl) * 128 + 64 * hv + g * 16) : 0x80000000u;
      mo = L_STG + (u32)((nl & 7) * MROW + 4 * g) * 4;
      mska = L_MSK + nl * 4;
    } else {
      hra = L_PD + (pi < 120 ? pn * 16 + (pc3 ? pc3 * 4 + 4 : 0) : 0);
      oo = pi < nn * 3 && !(lane & 1) ? (u32)(NN * 128 + (n0 * 3 + pi) * 2) : 0x80000000u;
      mo = L_STG + PART_P + (u32)((pn & 7) * 48 + pc3 * 16 + (lane & 1) * 404) * 4;
      mska = L_MSK + pn * 4;
    }
    fa = stg + (u32)((lane >> 5) * MROW + (lane & 31)) * 4; fp = stg + PART_P + (u32)((lane >> 5) * 48 + ((lane >> 4) & 1) * 404 + (lane & 15)) * 4;
  }
  asm volatile("" : "+v"(taba), "+v"(flea), "+v"(segbase), "+v"(rep4), "+v"(dummy), "+v"(ha), "+v"(hra), "+v"(wa), "+v"(wa2), "+v"(oo),
               "+v"(mo), "+v"(mska), "+v"(fa), "+v"(fp));
  const u32x4 psrd = make_srd(ka[1], 0, NN * 6), srd = make_srd(kb[0] + WS_BP, 128, NN), osrd = make_srd(kb[1], 0, NN * 134);
  int pslot = w + (c >> 3); pslot -= pslot >= 24 ? 24 : 0; pslot -= pslot >= 12 ? 12 : 0; pslot -= pslot >= 12 ? 12 : 0;
  const u32 padoff = ((u32)c * CHS + CH) * 8, bkey = (u32)b << 24;
  asm volatile("" ::"s"(psrd), "s"(srd), "s"(osrd), "s"(pslot), "s"(padoff), "s"(bkey));
  if (LIKELY(w < 10)) {
    u32 bk[4], rk[4];
    WAITV(4); asm volatile("" : "+v"(d0), "+v"(d1));
    u32 d[4] = {d0[0], d0[1], d1[0], d1[1]};
#pragma unroll
    for (int k = 0; k < 4; ++k) bk[k] = __umul24(d[k], 214749u) >> 23;
#pragma unroll
    for (int k = 0; k < 4; ++k) rk[k] = lds_add(L_HIST + bk[k] * 4, k < 2 || act1 ? 1u : 0u);
    arrive(SY0);
#pragma unroll
    for (int k = 0; k < 4; ++k) d[k] = ((d[k] - (u32)first_node(bk[k])) << 18) | (bk[k] << 24) | (k < 2 || act1 ? 0u : 0xfc0000u);
    asm volatile("" : "+v"(d[0]), "+v"(d[1]), "+v"(d[2]), "+v"(d[3]));
    await(SY0, 10);
    const u32x4 cn = lds_ld<u32x4>(L_HIST + lane * 16);
    const u32 tot = cn[0] + cn[1] + cn[2] + cn[3];
    const u32 ex = scan_add(tot) - tot;
    lds_st<u32x4>(L_OFFS + lane * 16, u32x4{ex, ex + cn[0], ex + cn[0] + cn[1], ex + cn[0] + cn[1] + cn[2]});
    asm volatile("" ::: "memory");
#pragma unroll
    for (int k = 0; k < 4; ++k) { rk[k] += lds_ld<u32>(L_OFFS + bk[k] * 4); if (k >= 2) rk[k] = umin(rk[k] + (act1 ? 0u : 65536u), CH + (k & 1)); rk[k] *= 8; }
    WAITV(2); asm volatile("" : "+v"(s0), "+v"(s1));
    const u32 s[4] = {s0[0], s0[1], s1[0], s1[1]};
#pragma unroll
    for (int k = 0; k < 4; ++k) lds_st<u32>(L_SIDX + (rk[k] >> 1), s[k] | d[k]);
    WAITV(1); asm volatile("" : "+v"(e0));
    lds_st<u32x2>(L_SEA + rk[0], u32x2{e0[0], e0[1]}); lds_st<u32x2>(L_SEA + rk[1], u32x2{e0[2], e0[3]});
    WAITV(0); asm volatile("" : "+v"(e1));
    lds_st<u32x2>(L_SEA + rk[2], u32x2{e1[0], e1[1]}); lds_st<u32x2>(L_SEA + rk[3], u32x2{e1[2], e1[3]});
    arrive(SY1A);
    await(SY1A, 10);
    st16_dev(ws, WS_IDX + (u32)c * CHS * 4 + tid * 16, lds_ld<u32x4>(L_SIDX + tid * 16));
    u32x4 x = lds_ld<u32x4>(L_SEA + tid * 16), y = lds_ld<u32x4>(L_SEA + 10240 + tid * 16);
    u32x4 t0 = {(ex | (cn[0] << 12)) | tag8, tag, ((ex + cn[0]) | (cn[1] << 12)) | tag8, tag};
    u32x4 t1 = {((ex + cn[0] + cn[1]) | (cn[2] << 12)) | tag8, tag, ((ex + tot - cn[3]) | (cn[3] << 12)) | tag8, tag};
    u32 ta = WS_TAB + (u32)c * 2048 + lane * 16, eao = WS_EA + (u32)c * CHS * 8 + tid * 16;
    asm volatile("s_waitcnt vmcnt(0)" : "+v"(x), "+v"(y), "+v"(t0), "+v"(t1), "+v"(ta), "+v"(eao)::"memory");
    if (ticket(SY1B, 1u) == 9) {
      st16_dev(ws, ta, t0);
      st16_dev(ws + 1024, ta, t1);
      arrive(SY1B);
    }
    await(SY1B, 11);
    pos_pieces(psrd, lane * 16 + (u32)pslot * 5120, (u32)pslot * 5120);
    WAITV(0);
    st16_dev(ws, eao, x);
    st16_dev(ws + 10240, eao, y);
  } else {
    const int m = 11 - w;
    const GAS u16* pp = m ? pos + (n0 + (lane < nn ? lane : 0)) * 3 : we + 128 * 64 + lane;
    u32 pv[5];
#pragma unroll
    for (int i = 0; i < 5; ++i) pv[i] = pp[m ? (i < 3 ? i : 0) : 64 * i];
    const u32 wxv = wx[lane];
    __builtin_amdgcn_s_waitcnt(0x0F70);
    WAITV(0); asm volatile("" : "+v"(d0), "+v"(d1), "+v"(s0), "+v"(s1), "+v"(e0), "+v"(e1));
    arrive(SYH);
    await(SYH, 2);
    const u32 ro16 = (u32)((lane & 15) * 128 + (lane >> 4) * 16);
    u32x4 hf[3][2];
#pragma unroll
    for (int t = 0; t < 3; ++t) { hf[t][0] = lds_ld<u32x4>(L_HR + t * 2048 + ro16); hf[t][1] = lds_ld<u32x4>(L_HR + t * 2048 + ro16 + 64); }
    const u32 a0 = L_WE + 8192 * m + (u32)((8 * (lane >> 4) + ((lane >> 2) & 3)) * 128 + 16 * (lane & 3));
    u32x4 v[2][3];
#pragma unroll
    for (int hv = 0; hv < 2; ++hv) {
      bf16x8 wfr[2][2];
#pragma unroll
      for (int T = 0; T < 2; ++T)
#pragma unroll
        for (int ks = 0; ks < 2; ++ks) { const u32 a = a0 + 4096 * ks + 64 * hv + 8 * T; wfr[T][ks] = as_frag(tr_read(a), tr_read(a + 512)); }
#pragma unroll
      for (int t = 0; t < 3; ++t)
#pragma unroll
        for (int T = 0; T < 2; ++T) {
          f32x4 acc = {0.f, 0.f, 0.f, 0.f};
          acc = MFMA16(wfr[T][0], as_frag(hf[t][0]), acc); acc = MFMA16(wfr[T][1], as_frag(hf[t][1]), acc);
          v[hv][t][2 * T] = pk2(acc[0] * NLOG2E, acc[1] * NLOG2E); v[hv][t][2 * T + 1] = pk2(acc[2] * NLOG2E, acc[3] * NLOG2E);
        }
    }
    if (m) {
#pragma unroll
      for (int t = 0; t < 3; ++t) {
        if (t < 2 || 32 + (lane & 15) < nn) { st16_dev(ws + WS_BP + t * 2048, (u32)n0 * 128 + ro16, v[0][t]); st16_dev(ws + WS_BP + t * 2048 + 64, (u32)n0 * 128 + ro16, v[1][t]); }
      }
      if (lane < 40) lds_st<u32x4>(L_PD + lane * 16, u32x4{pv[0] << 16, (L_OH - POFF + (lane & 7) * 16) << 16, pv[1] << 16, pv[2] << 16});
      lds_st<u32>(L_WX + lane * 4, wxv << 16);
    } else {
#pragma unroll
      for (int t = 0; t < 3; ++t) {
        lds_st<u32x4>(L_G + t * 2048 + ro16, v[0][t]); lds_st<u32x4>(L_G + t * 2048 + ro16 + 64, v[1][t]);
      }
#pragma unroll
      for (int r = 0; r < 8; ++r) lds_st<u16>(L_G + (48 + r) * 128 + lane * 2, r < 4 ? (u16)pk2(b2f(pv[1 + r]) * NLOG2E, 0.f) : (r == 5 ? (u16)pv[0] : (u16)0));
    }
    await(SY1B, 11);
    pos_pieces(psrd, lane * 16 + (u32)pslot * 5120, (u32)pslot * 5120);
  }
  asm volatile("s_waitcnt vmcnt(0)" ::: "memory");
  if (ticket(SY2, 1u) == NW - 1) st16_dev(ws + WS_FLE, (u32)c * 1024 + lane * 16, u32x4{~tag, tag, ~tag, tag});
  u64 g;
  await(SY2, NW);
  g = poll64(ws + WS_TAB, taba);

  if (UNLIKELY((BALLOT((g >> 24) != gtag) & svm) != 0)) {
#pragma unroll 1
    for (u32 spins = 0; spins < SPIN_CAP; ++spins) {
      g = poll64(ws + WS_TAB, taba);
      if ((BALLOT((g >> 24) != gtag) & svm) == 0) break;
      g = 0;
      __builtin_amdgcn_s_sleep(2);
    }
  }
  const u32 te = sv ? (u32)g & 0xffffffu : 0u;
  u32x2 ridx[PASSES];
  u32 ro[PASSES], Tw, nv;
  u64 on[PASSES];
  flatten<PASSES>(te, segbase, lane, padoff, dsc, 0u, ro, on, Tw, nv);
  fetch_index<PASSES>(ws, ro, ridx);
  u64 fle;
  const u32 rbase = ticket(L_FLG + 4, 2 * nv);
  const bool rok = rbase + 2 * nv <= (u32)RCAP;
  if (UNLIKELY(Tw > (u32)QFAST || !rok)) lds_st<u32>(L_FLG, 1u);
  const u32 eptr = L_EA - POFF + rbase * 8 + lane * 16;
  WAITV(0);
  asm volatile("global_load_dwordx2 %0, %1, %2 sc1" : "=&v"(fle) : "v"(flea), "s"(ws + WS_FLE) : "memory");
#pragma unroll
  for (int k = 0; k < PASSES; ++k) asm volatile("" : "+v"(ridx[k]));
  u32 rrank[4], rdvo[4], ca[4], rrank2[2], rdvo2[2], ca2[2];
  u32x2 sent[4], rdv[4], sent2[2], rdv2[2];
  rank_records<2>(rep4, dummy, bkey, ridx, rrank, ca);
  if (UNLIKELY(nv > 128)) rank_records<1>(rep4, dummy, bkey, ridx + 2, rrank2, ca2);
  arrive(SY5);
  {
    u32 a6[4];
    u32x2 ps[4];
    u32x4 pd[4];
    load_geometry<2>(ridx, ca, a6, ps, pd);
    geometry<2>(eptr, ridx, ca, a6, ps, pd, rrank, rdvo, sent, rdv);
  }
  if (UNLIKELY(nv > 128)) {
    u32 a6[2];
    u32x2 ps[2];
    u32x4 pd[2];
    load_geometry<1>(ridx + 2, ca2, a6, ps, pd);
    geometry<1>(eptr + 2048, ridx + 2, ca2, a6, ps, pd, rrank2, rdvo2, sent2, rdv2);
  }
  WAITV(0);
  asm volatile("" : "+v"(fle));
#pragma unroll 1
  for (u32 spins = 0; UNLIKELY((BALLOT(fle != ftag) & svm) != 0) && spins < SPIN_CAP; ++spins) fle = poll64(ws + WS_FLE, flea);
  if (rok) fetch_attrs<PASSES>(ws, ro, on, rbase);
  await(SY5, NW);
  const u32x4 cnt = lds_ld<u32x4>(L_CNT + lane * 16);
  const u32 flg = lds_ld<u32>(L_FLG);
  u32 nstart, total;
  scan_nodes(cnt, lane, nstart, total);
  bool rare = __builtin_amdgcn_readfirstlane(flg) != 0 || total > (u32)CAP;
  u32 sbeg = 0, send = 0;
  int fn = 0, ln = 0;
  if (LIKELY(!rare)) {
    scatter_records<2>(rrank, rdvo, ca, sent);
    if (UNLIKELY(nv > 128)) scatter_records<1>(rrank2, rdvo2, ca2, sent2);
    finish_lists(lane, w, nstart, total, sbeg, send);
    W.s4a += (sbeg & ~31u) * 8;
    scatter_diffs<2>(rdvo, rdv);
    if (UNLIKELY(nv > 128)) scatter_diffs<1>(rdvo2, rdv2);
    fn = min((int)__builtin_popcountll(BALLOT(nstart <= sbeg)), nn) - 1;
    ln = __builtin_popcountll(BALLOT(nstart < send)) - 1;
    if (UNLIKELY(ln - fn >= 8 || sbeg + 32 >= send)) lds_st<u32>(L_FLG + 8, 1u);
    if ((u32)(lane - fn) <= (u32)(ln - fn)) __hip_atomic_fetch_or((LAS u32*)(size_t)(L_MSK + lane * 4), 1u << w, __ATOMIC_RELAXED, __HIP_MEMORY_SCOPE_WORKGROUP);
    setup_window(W, nn, fn, sbeg);
    WAITV(0);
    wg_barrier();
    first_tile(W, stg, srd);
    rare = __builtin_amdgcn_readfirstlane(lds_ld<u32>(L_FLG + 8)) != 0;
  }
  if (LIKELY(!rare)) {
    u32 mk = lds_ld<u32>(mska), ma[2];
    float wx0 = lds_ld<float>(L_WX + (lane & 31) * 4), wx1 = lds_ld<float>(L_WX + 128 + (lane & 31) * 4);
#pragma unroll
    for (int u = 0; u < 2; ++u) { ma[u] = mk ? (u32)__builtin_ctz(mk) * STGW + mo : L_ZERO; mk &= mk - 1; }
    const bool more = BALLOT(mk != 0) != 0;
    asm volatile("" : "+v"(ma[0]), "+v"(ma[1]), "+v"(mk), "+v"(wx0), "+v"(wx1));
    u32 rem;
    const float rmin = run_tiles<true>(W, stg, srd, sbeg, send, wh, w, lane * 16, SY6, rem);
    if (UNLIKELY(BALLOT(!(rmin > 1e-36f)))) {
      lane = __builtin_amdgcn_mbcnt_hi(~0u, __builtin_amdgcn_mbcnt_lo(~0u, 0u));
      asm volatile("" : "+v"(lane));
      init_window(W, lane, stg);
      W.s4a += (sbeg & ~31u) * 8;
      setup_window(W, nn, fn, sbeg);
      first_tile(W, stg, srd);
      run_tiles<false>(W, stg, srd, sbeg, send, wh, w, lane * 16, rem ? SY6 : SY7, rem);
    }
#pragma unroll
    for (int g4 = 0; g4 < 4; ++g4) {
      lds_st<u32>(fa + g4 * 2 * MROW * 4, pk2(W.acc0[4 * g4], W.acc1[4 * g4]));
      const f32x2 yz = f32x2{W.acc0[4 * g4 + 2], W.acc0[4 * g4 + 3]} * wx0 + f32x2{W.acc1[4 * g4 + 2], W.acc1[4 * g4 + 3]} * wx1;
      float xx = W.acc0[4 * g4 + 1] * wx0;
      asm("v_fmac_f32 %0, %1, %2" : "+v"(xx) : "v"(W.acc1[4 * g4 + 1]), "v"(wx1));
      lds_st<float>(fp + g4 * 384, xx); lds_st<float>(fp + g4 * 384 + 64, yz[0]); lds_st<float>(fp + g4 * 384 + 128, yz[1]);
    }
    if (w < 6) {
      Node nd;
      await(SY6, NW);
      node_hpart(nd, ha, hra, wa, wa2);
      wg_barrier();
      const u32x4 m00 = lds_ld<u32x4>(ma[0]), m01 = lds_ld<u32x4>(ma[0] + 64), m10 = lds_ld<u32x4>(ma[1]), m11 = lds_ld<u32x4>(ma[1] + 64);
      __builtin_amdgcn_sched_barrier(0);
      node_msg(nd, m00, m01);
      node_msg(nd, m10, m11);
      if (UNLIKELY(more)) {
#pragma unroll 1
        do {
          const u32 a = mk ? (u32)__builtin_ctz(mk) * STGW + mo : L_ZERO;
          mk &= mk - 1;
          node_msg(nd, lds_ld<u32x4>(a), lds_ld<u32x4>(a + 64));
        } while (BALLOT(mk != 0));
      }
      put_node(nd, osrd, oo);
    } else {
      float pold = lds_ld<float>(hra);
      asm volatile("" : "+v"(pold));
      wg_barrier();
      if (pw < 4) {
        f32x4 pv[8];
#pragma unroll
        for (int i = 0; i < 8; ++i) pv[i] = lds_ld<f32x4>(ma[i >> 2] + 16 * (i & 3));
        __builtin_amdgcn_sched_barrier(0);
        f32x4 pp = ((pv[0] + pv[1]) + (pv[2] + pv[3])) + ((pv[4] + pv[5]) + (pv[6] + pv[7]));
        if (UNLIKELY(more)) {
#pragma unroll 1
          do {
            pp += sum16(mk ? (u32)__builtin_ctz(mk) * STGW + mo : L_ZERO);
            mk &= mk - 1;
          } while (BALLOT(mk != 0));
        }
        put_pos(pp, pold, osrd, oo);
      }
    }
  } else {
    for (u32 i = w * 64 + lane; i < ACCS / 16; i += NW * 64) st16_dev(ws, WS_ACC + (u32)c * ACCS + i * 16, u32x4{0, 0, 0, 0});
    if (lane == 0) __hip_atomic_fetch_max((LAS u32*)(size_t)(L_FLG + 12), Tw, __ATOMIC_RELAXED, __HIP_MEMORY_SCOPE_WORKGROUP);
    GAS float* ga = (GAS float*)(ws + WS_ACC + (u32)c * ACCS);
#pragma unroll 1
    for (u32 vbase = 0;; vbase += 64) {
      asm volatile("s_waitcnt vmcnt(0)" ::: "memory");
      wg_barrier();
      asm volatile("" : "+v"(lane));
      if (vbase >= (u32)__builtin_amdgcn_readfirstlane(lds_ld<u32>(L_FLG + 12))) break;
      if (w == 0) { lds_st<u32x4>(L_CNT + lane * 16, u32x4{0, 0, 0, 0}); lds_st<u32>(L_CNT + 1024 + lane * 4, 0u); lds_st<u32>(L_FLG + 4, 0u); }
      lds_st<u32>(dsc + lane * 4, 0u);
      pos_pieces(psrd, lane * 16 + (u32)w * 5120, (u32)w * 5120);
      wg_barrier();
      u32x2 ridx1[1], sent1[2], rdv1[2];
      u64 on1[1];
      u32 ro1[1], rrank1[2], ca1[2], Tw1, nv1, sb, se, nst, tot;
      flatten<1>(te, (u32)(seg0 + lane) * CHS * 8, lane, padoff, dsc, vbase, ro1, on1, Tw1, nv1);
      const u32 rbase1 = ticket(L_FLG + 4, 2 * nv1);
      fetch_index<1>(ws, ro1, ridx1);
      fetch_attrs<1>(ws, ro1, on1, rbase1);
      WAITV(0); asm volatile("" : "+v"(ridx1[0]));
      rank_records<1>((lane & 3) * 4, 1024u + lane * 4, bkey, ridx1, rrank1, ca1);
      wg_barrier();
      u32 a61[2], rdvo1[2];
      u32x2 ps1[2];
      u32x4 pd1[2];
      load_geometry<1>(ridx1, ca1, a61, ps1, pd1);
      geometry<1>(L_EA - POFF + rbase1 * 8 + lane * 16, ridx1, ca1, a61, ps1, pd1, rrank1, rdvo1, sent1, rdv1);
      scan_nodes(lds_ld<u32x4>(L_CNT + lane * 16), lane, nst, tot);
      scatter_records<1>(rrank1, rdvo1, ca1, sent1);
      scatter_diffs<1>(rdvo1, rdv1);
      finish_lists(lane, w, nst, tot, sb, se);
      wg_barrier();
#pragma unroll 1
      while (sb < se) {
        asm volatile("" : "+v"(lane));
        const int base = __builtin_popcountll(__ballot(nst <= sb && lane < nn)) - 1;
        const u32 wend = umin(__builtin_amdgcn_readlane(nst, base + 8 < nn ? base + 8 : nn), se);
        Win V;
        init_window(V, lane, stg);
        V.s4a += (sb & ~31u) * 8;
        setup_window(V, nn, base, sb);
        first_tile(V, stg, srd);
        u32 rem;
        run_tiles<false>(V, stg, srd, sb, wend, wh, w, lane * 16, SY7, rem);
        asm volatile("" : "+v"(lane));
        const float wx0 = lds_ld<float>(L_WX + (lane & 31) * 4), wx1 = lds_ld<float>(L_WX + 128 + (lane & 31) * 4);
        const int lastn = __builtin_popcountll(__ballot(nst < wend && lane < nn)) - 1;
#pragma unroll
        for (int g4 = 0; g4 < 4; ++g4) {
          float pv[3];
#pragma unroll
          for (int cc = 0; cc < 3; ++cc) {
            const float v = V.acc0[4 * g4 + 1 + cc] * wx0 + V.acc1[4 * g4 + 1 + cc] * wx1;
            pv[cc] = v + dppf<0xB1>(v);
          }
          const int nd = base + ((2 * g4 + (lane >> 5) - base) & 7);
          if (nd <= lastn) {
            __hip_atomic_fetch_add(ga + nd * 112 + (lane & 31), V.acc0[4 * g4], RLX_AGENT);
            __hip_atomic_fetch_add(ga + nd * 112 + 32 + (lane & 31), V.acc1[4 * g4], RLX_AGENT);
            if (!(lane & 1)) {
#pragma unroll
              for (int cc = 0; cc < 3; ++cc) __hip_atomic_fetch_add(ga + nd * 112 + 64 + 16 * cc + ((lane & 31) >> 1), pv[cc], RLX_AGENT);
            }
          }
        }
        sb = wend;
      }
    }
    load_wh(wh, w, lane);
    asm volatile("s_waitcnt vmcnt(0)" ::: "memory");
    wg_barrier();
    if (w < 6) {
      Node nd;
      node_hpart(nd, ha, hra, wa, wa2);
      const int nl = (w >= 3 ? w - 3 : w) * 16 + (lane & 15), nq = (nl < 40 ? nl : 0) * 112 + 4 * (lane >> 4);
      u32x4 mf[2];
#pragma unroll
      for (int i = 0; i < 4; ++i)
#pragma unroll
        for (int k = 0; k < 2; ++k) mf[k][i] = pk2(__hip_atomic_load(ga + nq + 16 * k + i, RLX_AGENT), __hip_atomic_load(ga + nq + 16 * k + 32 + i, RLX_AGENT));
      node_msg(nd, mf[0], mf[1]);
      put_node(nd, osrd, oo);
    } else {
      const int pi = pw * 32 + (lane >> 1), pn = pi < 120 ? pi / 3 : 0, pq = pn * 112 + 64 + (pi < 120 ? pi - pn * 3 : 0) * 16 + (lane & 1) * 8;
      f32x4 pp;
#pragma unroll
      for (int i = 0; i < 4; ++i) pp[i] = __hip_atomic_load(ga + pq + i, RLX_AGENT) + __hip_atomic_load(ga + pq + 4 + i, RLX_AGENT);
      put_pos(pp, lds_ld<float>(hra), osrd, oo);
    }
  }
  if (c == 0 && tid == 0) __hip_atomic_store((GAS u32*)(ws + WS_EP), tag, RLX_AGENT);
}

__global__ void egnn_g_pre(const u16* h, const u16* we, float* A, float* B, float* M, float* P, int D) {
  const int n = blockIdx.x;
  for (int f = threadIdx.x; f < D; f += 64) {
    float a = 0.f, b = 0.f;
    for (int k = 0; k < D; ++k) { const float x = b2f(h[(size_t)n * D + k]); a += x * b2f(we[k * D + f]); b += x * b2f(we[(D + k) * D + f]); }
    A[(size_t)n * D + f] = a; B[(size_t)n * D + f] = b; M[(size_t)n * D + f] = 0.f;
  }
  if (threadIdx.x < 4) P[n * 4 + threadIdx.x] = 0.f;
}
__global__ void egnn_g_edge(const float* A, const float* B, const u16* pos, const u16* ea, const int* src, const int* dst, const u16* we,
                            const u16* wx, float* M, float* P, int E, int D, int De) {
  const int e = blockIdx.x * 4 + (threadIdx.x >> 6), lane = threadIdx.x & 63;
  if (e >= E) return;
  const int s = src[e], d = dst[e];
  const float dx = b2f(pos[d * 3]) - b2f(pos[s * 3]), dy = b2f(pos[d * 3 + 1]) - b2f(pos[s * 3 + 1]), dz = b2f(pos[d * 3 + 2]) - b2f(pos[s * 3 + 2]);
  const float d2 = dx * dx + dy * dy + dz * dz;
  float cs = 0.f;
  for (int f = lane; f < D; f += 64) {
    float x = A[(size_t)d * D + f] + B[(size_t)s * D + f] + d2 * b2f(we[2 * D * D + f]);
    for (int k = 0; k < De; ++k) x += b2f(ea[(size_t)e * De + k]) * b2f(we[(2 * D + 1 + k) * D + f]);
    const float m = x / (1.f + __expf(-x));
    atomicAdd(&M[(size_t)d * D + f], m);
    cs += m * b2f(wx[f]);
  }
  for (int o = 32; o; o >>= 1) cs += __shfl_xor(cs, o);
  if (lane < 3) atomicAdd(&P[d * 4 + lane], (lane == 0 ? dx : (lane == 1 ? dy : dz)) * cs);
}
__global__ void egnn_g_node(const u16* h, const u16* pos, const u16* wh, const float* M, const float* P, u16* out, int N, int D) {
  const int n = blockIdx.x;
  for (int f = threadIdx.x; f < D; f += 64) {
    float z = 0.f;
    for (int k = 0; k < D; ++k) z += b2f(h[(size_t)n * D + k]) * b2f(wh[k * D + f]) + M[(size_t)n * D + k] * b2f(wh[(D + k) * D + f]);
    out[(size_t)n * D + f] = (u16)pk2(b2f(h[(size_t)n * D + f]) + z / (1.f + __expf(-z)), 0.f);
  }
  if (threadIdx.x < 3) out[(size_t)N * D + n * 3 + threadIdx.x] = (u16)pk2(b2f(pos[n * 3 + threadIdx.x]) + P[n * 4 + threadIdx.x], 0.f);
}

extern "C" void kernel_launch(void* const* d_in, const int* in_sizes, int n_in, void* d_out, int out_size, void* d_ws, size_t ws_size,
                              hipStream_t stream) {
  const int N = in_sizes[1] / 3, E = in_sizes[3], D = in_sizes[6], De = E > 0 ? in_sizes[2] / E : 0;
  if (N == NN && E == NBK * CH && D == 64 && De == 4 && ws_size >= WS_END) {
    (void)hipFuncSetAttribute((const void*)egnn_fused, hipFuncAttributeMaxDynamicSharedMemorySize, (int)L_END);
    egnn_fused<<<NBK, NW * 64, L_END, stream>>>((const u16*)d_in[0], (const u16*)d_in[1], d_in[2], d_in[3], d_in[4], (const u16*)d_in[5],
                                                (const u16*)d_in[6], (const u16*)d_in[7], (char*)d_ws, (u16*)d_out);
  } else {
    float* A = (float*)d_ws; float* B = A + (size_t)N * D; float* M = B + (size_t)N * D; float* P = M + (size_t)N * D;
    egnn_g_pre<<<N, 64, 0, stream>>>((const u16*)d_in[0], (const u16*)d_in[5], A, B, M, P, D);
    if (E > 0)
      egnn_g_edge<<<(E + 3) / 4, 256, 0, stream>>>(A, B, (const u16*)d_in[1], (const u16*)d_in[2], (const int*)d_in[3], (const int*)d_in[4],
                                                   (const u16*)d_in[5], (const u16*)d_in[6], M, P, E, D, De);
    egnn_g_node<<<N, 64, 0, stream>>>((const u16*)d_in[0], (const u16*)d_in[1], (const u16*)d_in[7], M, P, (u16*)d_out, N, D);
  }
}
```
